```python
import torch

def _softmax(x, axis=-1):
    return torch.softmax(x, dim=axis)
def _gelu(x):
    return torch.nn.functional.gelu(x, approximate="tanh")
def _silu(x):
    return torch.nn.functional.silu(x)
def _sigmoid(x):
    return torch.sigmoid(x)
def _layernorm(x, axis=-1, eps=1e-5):
    m = x.mean(dim=axis, keepdim=True)
    v = x.var(dim=axis, keepdim=True, unbiased=False)
    return (x - m) / torch.sqrt(v + eps)
def _scatter_add(tgt, idx, src, axis=0):
    return tgt.clone().index_add_(axis, idx, src)
def _segment_sum(src, seg, n):
    out = torch.zeros((n,) + src.shape[1:], dtype=src.dtype)
    return out.index_add_(0, seg, src)
def _pad(x, pw):
    flat = tuple(v for pair in reversed(pw) for v in pair)
    return torch.nn.functional.pad(x, flat)

kernel_name = 'parallel_sum__linear_kernel_hedgehog__mixer_token_mlp__prompt'

def setup_inputs():

    _w = (lambda a: (lambda t: t.to(torch.bfloat16) if t.is_floating_point() else t)(torch.as_tensor(a)))
    def _A_setup():
        B, S, D, H = 1, 2048, 512, 32
        Dh = D // H
        return {
            "x": _w((lambda *s: torch.randn(*s, dtype=torch.bfloat16) * 0.05)(B, S, D)),
            "wqkv": _w((lambda *s: torch.randn(*s, dtype=torch.bfloat16) * 0.2)(D, 3 * D)),
            "w_phi": _w((lambda *s: torch.randn(*s, dtype=torch.bfloat16) * 0.2)(H, Dh, Dh) * (Dh ** -0.5)),
            "wo": _w((lambda *s: torch.randn(*s, dtype=torch.bfloat16) * 0.05)(D, D)),
        }
    _A_in = _A_setup()

    def _B_setup():
        B, S, D, F = 1, 2048, 512, 1376
        return {
            "x": _w((lambda *s: torch.randn(*s, dtype=torch.bfloat16) * 0.05)(B, S, D)),
            "w1": _w((lambda *s: torch.randn(*s, dtype=torch.bfloat16) * 0.8)(S, F) * (S ** -0.5)),
            "b1": _w((lambda *s: torch.randn(*s, dtype=torch.bfloat16) * 3.2)(F) * 0.02),
            "w2": _w((lambda *s: torch.randn(*s, dtype=torch.bfloat16) * 0.4)(F, S) * (F ** -0.5)),
            "b2": _w((lambda *s: torch.randn(*s, dtype=torch.bfloat16) * 1.6)(S) * 0.02),
        }
    _B_in = _B_setup()

    return {
        "A_x": _A_in["x"],
        "A_wqkv": _A_in["wqkv"],
        "A_w_phi": _A_in["w_phi"],
        "A_wo": _A_in["wo"],
        "B_x": _B_in["x"],
        "B_w1": _B_in["w1"],
        "B_b1": _B_in["b1"],
        "B_w2": _B_in["w2"],
        "B_b2": _B_in["b2"],
    }

def reference(A_x, A_wqkv, A_w_phi, A_wo, B_x, B_w1, B_b1, B_w2, B_b2):
    def _A_ref(x, wqkv, w_phi, wo):
        B, S, D = x.shape
        H = 32; Dh = D // H
        qkv = (lambda x, *d: x.permute(*d))((x @ wqkv).reshape(B, S, 3, H, Dh), 2, 0, 3, 1, 4)
        q, k, v = qkv[0], qkv[1], qkv[2]
        pq = torch.einsum("bhsd,hdf->bhsf", q, w_phi)
        pk = torch.einsum("bhsd,hdf->bhsf", k, w_phi)
        phi_q = torch.concatenate([_softmax(pq, axis=-1), _softmax(-pq, axis=-1)], axis=-1)
        phi_k = torch.concatenate([_softmax(pk, axis=-1), _softmax(-pk, axis=-1)], axis=-1)
        kv = torch.einsum("bhsf,bhsd->bhfd", phi_k, v)
        z  = torch.einsum("bhsf->bhf", phi_k)
        num = torch.einsum("bhqf,bhfd->bhqd", phi_q, kv)
        den = torch.einsum("bhqf,bhf->bhq", phi_q, z)[..., None] + 1e-6
        o = num / den
        return (lambda x, *d: x.permute(*d))(o, 0, 2, 1, 3).reshape(B, S, D) @ wo

    def _B_ref(x, w1, b1, w2, b2):
        B, S, D = x.shape
        h = _layernorm(x, axis=-1)
        u = torch.einsum("bsd,sf->bdf", h, w1) + b1
        u = _gelu(u)
        y = torch.einsum("bdf,fs->bds", u, w2) + b2
        return (lambda x, *d: x.permute(*d))(y, 0, 2, 1)

    x = A_x
    xn = _layernorm(x, axis=-1)
    return x + _A_ref(xn, A_wqkv, A_w_phi, A_wo) + _B_ref(xn, B_w1, B_b1, B_w2, B_b2)
```

```cpp
#include <hip/hip_runtime.h>
#include <hip/hip_bf16.h>
typedef __hip_bfloat16 bf16;
constexpr int S=2048, D=512, H=32, DH=16, F=1376;
__device__ inline float b2f(bf16 v){return __bfloat162float(v);}
__global__ void k_ln(const bf16* x, float* xn, float* h){
  int s=blockIdx.x; int t=threadIdx.x;
  float v[8]; float sum=0;
  for(int i=0;i<8;i++){v[i]=b2f(x[s*D+t*8+i]); sum+=v[i];}
  for(int o=32;o>0;o>>=1) sum+=__shfl_xor(sum,o);
  float m=sum/D; float q=0;
  for(int i=0;i<8;i++){float d=v[i]-m; q+=d*d;}
  for(int o=32;o>0;o>>=1) q+=__shfl_xor(q,o);
  float r=rsqrtf(q/D+1e-5f);
  sum=0;
  for(int i=0;i<8;i++){v[i]=(v[i]-m)*r; xn[s*D+t*8+i]=v[i]; sum+=v[i];}
  for(int o=32;o>0;o>>=1) sum+=__shfl_xor(sum,o);
  m=sum/D; q=0;
  for(int i=0;i<8;i++){float d=v[i]-m; q+=d*d;}
  for(int o=32;o>0;o>>=1) q+=__shfl_xor(q,o);
  r=rsqrtf(q/D+1e-5f);
  for(int i=0;i<8;i++){h[s*D+t*8+i]=(v[i]-m)*r;}
}
__global__ void k_qkv(const float* xn, const bf16* w, float* qkv){
  int n=blockIdx.x*256+threadIdx.x; int s0=blockIdx.y*8;
  float acc[8]={};
  for(int d=0;d<D;d++){float wv=b2f(w[d*1536+n]); for(int i=0;i<8;i++) acc[i]+=xn[(s0+i)*D+d]*wv;}
  for(int i=0;i<8;i++) qkv[(s0+i)*1536+n]=acc[i];
}
__global__ void k_phi(const float* qkv, const bf16* wphi, float* phi){
  int idx=blockIdx.x*256+threadIdx.x;
  int h=idx%H; int s=(idx/H)%S; int t=idx/(H*S);
  float p[16];
  for(int f=0;f<16;f++){float a=0; for(int d=0;d<16;d++) a+=qkv[s*1536+t*512+h*16+d]*b2f(wphi[h*256+d*16+f]); p[f]=a;}
  float mx=p[0],mn=p[0];
  for(int f=1;f<16;f++){mx=fmaxf(mx,p[f]); mn=fminf(mn,p[f]);}
  float e1[16],e2[16],s1=0,s2=0;
  for(int f=0;f<16;f++){e1[f]=__expf(p[f]-mx); s1+=e1[f]; e2[f]=__expf(mn-p[f]); s2+=e2[f];}
  float* o=phi+(size_t)idx*32;
  for(int f=0;f<16;f++){o[f]=e1[f]/s1; o[16+f]=e2[f]/s2;}
}
__global__ void k_kv(const float* phi, const float* qkv, float* kv){
  int h=blockIdx.x; int t=threadIdx.x;
  int f=t/17, d=t%17;
  float a=0;
  for(int s=0;s<S;s++){
    float pk=phi[((size_t)(S+s)*H+h)*32+f];
    float v=d<16?qkv[s*1536+1024+h*16+d]:1.f;
    a+=pk*v;
  }
  kv[(h*32+f)*17+d]=a;
}
__global__ void k_o(const float* phi, const float* kv, float* o){
  int idx=blockIdx.x*256+threadIdx.x;
  int s=idx/512, c=idx%512, h=c/16, d=c%16;
  float num=0,den=0;
  for(int f=0;f<32;f++){float p=phi[((size_t)s*H+h)*32+f]; num+=p*kv[(h*32+f)*17+d]; den+=p*kv[(h*32+f)*17+16];}
  o[idx]=num/(den+1e-6f);
}
__global__ void k_u(const float* h, const bf16* w1, const bf16* b1, float* u){
  int f=blockIdx.x*32+(threadIdx.x&31); int d0=blockIdx.y*64+(threadIdx.x>>5)*8;
  float acc[8]={};
  for(int s=0;s<S;s++){float wv=b2f(w1[s*F+f]); for(int i=0;i<8;i++) acc[i]+=h[s*D+d0+i]*wv;}
  for(int i=0;i<8;i++){float x=acc[i]+b2f(b1[f]); float g=0.5f*x*(1.f+tanhf(0.7978845608f*(x+0.044715f*x*x*x))); u[(d0+i)*F+f]=g;}
}
__global__ void k_out(const bf16* x, const float* o, const bf16* wo, const float* u, const bf16* w2, const bf16* b2, bf16* out){
  int s=blockIdx.x*32+(threadIdx.x&31); int d0=blockIdx.y*64+(threadIdx.x>>5)*8;
  float acc[8]={};
  for(int f=0;f<F;f++){float wv=b2f(w2[f*S+s]); for(int i=0;i<8;i++) acc[i]+=u[(d0+i)*F+f]*wv;}
  for(int c=0;c<D;c++){float ov=o[s*D+c]; for(int i=0;i<8;i++) acc[i]+=ov*b2f(wo[c*D+d0+i]);}
  for(int i=0;i<8;i++) out[s*D+d0+i]=__float2bfloat16(acc[i]+b2f(b2[s])+b2f(x[s*D+d0+i]));
}
extern "C" void kernel_launch(void* const* d_in, const int* in_sizes, int n_in,
                              void* d_out, int out_size, void* d_ws, size_t ws_size,
                              hipStream_t stream) {
  const bf16* x=(const bf16*)d_in[0]; const bf16* wqkv=(const bf16*)d_in[1]; const bf16* wphi=(const bf16*)d_in[2];
  const bf16* wo=(const bf16*)d_in[3]; const bf16* w1=(const bf16*)d_in[5]; const bf16* b1=(const bf16*)d_in[6];
  const bf16* w2=(const bf16*)d_in[7]; const bf16* b2=(const bf16*)d_in[8];
  float* ws=(float*)d_ws;
  float* xn=ws; float* h=xn+S*D; float* qkv=h+S*D; float* phi=qkv+S*1536; float* kv=phi+(size_t)2*S*H*32; float* o=kv+32*32*17; float* u=o+S*D;
  k_ln<<<S,64,0,stream>>>(x,xn,h);
  k_qkv<<<dim3(6,S/8),256,0,stream>>>(xn,wqkv,qkv);
  k_phi<<<2*S*H/256,256,0,stream>>>(qkv,wphi,phi);
  k_kv<<<H,544,0,stream>>>(phi,qkv,kv);
  k_o<<<S*512/256,256,0,stream>>>(phi,kv,o);
  k_u<<<dim3(F/32,8),256,0,stream>>>(h,w1,b1,u);
  k_out<<<dim3(S/32,8),256,0,stream>>>(x,o,wo,u,w2,b2,(bf16*)d_out);
}
```

```cpp
#include <hip/hip_runtime.h>
#include <stdint.h>

#define GAS __attribute__((address_space(1)))
#define LAS __attribute__((address_space(3)))
#define DI __device__ __forceinline__
typedef short bf16x8 __attribute__((ext_vector_type(8)));
typedef short s16x4 __attribute__((ext_vector_type(4)));
typedef __bf16 bf2 __attribute__((ext_vector_type(2)));
typedef float f32x16 __attribute__((ext_vector_type(16)));
typedef float f32x4 __attribute__((ext_vector_type(4)));
typedef unsigned v4u __attribute__((ext_vector_type(4)));
typedef unsigned v2u __attribute__((ext_vector_type(2)));
typedef unsigned long long u64;
typedef GAS u64 gu64;

namespace hk {
constexpr unsigned REPB = 8192;
constexpr unsigned WS_HF = 4096, WS_KF = WS_HF + 2048, WS_OF = WS_KF + 1024, WS_UF = WS_OF + 1024;
constexpr unsigned WS_ZP = 131072, WS_KVP = 196608, WS_TS = 768 * 1024;
constexpr unsigned WS_H = 1u << 20, WS_U = 3u << 20, WS_O = 9u << 19, WS_BYTES = 13u << 19;
constexpr unsigned REG = 40960, ROW = 8192, STG = 49152, LDS_BYTES = 163840;
constexpr unsigned SPIN_CAP = 1u << 14;
constexpr int AUX_SC1 = 16;
constexpr float LOG2E = 1.4426950408889634f;
#define CODE_KB 0

struct Params {
  const char *x, *wqkv, *wphi, *wo, *w1, *b1, *w2, *b2;
  char *out, *ws;
};

DI f32x16 mfma(v4u a, v4u b, f32x16 c) {
  return __builtin_amdgcn_mfma_f32_32x32x16_bf16(__builtin_bit_cast(bf16x8, a), __builtin_bit_cast(bf16x8, b), c, 0, 0, 0);
}
DI unsigned pk(float a, float b) { bf2 v = {(__bf16)a, (__bf16)b}; return __builtin_bit_cast(unsigned, v); }
DI float bflo(unsigned u) { return __uint_as_float(u << 16); }
DI float bfhi(unsigned u) { return __uint_as_float(u & 0xffff0000u); }
template <int O> DI v4u pack8(const f32x16& x) {
  return v4u{pk(x[O], x[O + 1]), pk(x[O + 2], x[O + 3]), pk(x[O + 4], x[O + 5]), pk(x[O + 6], x[O + 7])};
}
DI v2u trr(unsigned a) { return __builtin_bit_cast(v2u, __builtin_amdgcn_ds_read_tr16_b64_v4i16((LAS s16x4*)(size_t)a)); }
DI v4u trfrag(unsigned a0, unsigned a1) { v2u lo = trr(a0), hi = trr(a1); return v4u{lo.x, lo.y, hi.x, hi.y}; }
DI v4u lds_r128(unsigned a) { return *(LAS v4u*)(size_t)a; }
DI v2u lds_r64(unsigned a) { return *(LAS v2u*)(size_t)a; }
DI float lds_r32(unsigned a) { return *(LAS float*)(size_t)a; }
DI void lds_w128(unsigned a, v4u v) { *(LAS v4u*)(size_t)a = v; }
DI void lds_w64(unsigned a, v2u v) { *(LAS v2u*)(size_t)a = v; }
DI void lds_w32(unsigned a, float v) { *(LAS float*)(size_t)a = v; }
DI void glds1(unsigned lds, const char* sb, unsigned v0) {
  asm volatile("s_mov_b32 m0, %0\n\ts_nop 0\n\tglobal_load_lds_dwordx4 %2, %1" ::"s"(lds), "s"(sb), "v"(v0) : "memory");
}
DI void glds2(unsigned lds, const char* sb, unsigned v0, unsigned v1) {
  asm volatile("s_mov_b32 m0, %0\n\ts_nop 0\n\tglobal_load_lds_dwordx4 %2, %1\n\tglobal_load_lds_dwordx4 %3, %1 offset:1024"
               ::"s"(lds), "s"(sb), "v"(v0), "v"(v1) : "memory");
}
DI void glds3(unsigned lds, const char* sb, unsigned v0, unsigned v1, unsigned v2) {
  asm volatile("s_mov_b32 m0, %0\n\ts_nop 0\n\tglobal_load_lds_dwordx4 %2, %1\n\tglobal_load_lds_dwordx4 %3, %1 offset:1024\n\t"
               "global_load_lds_dwordx4 %4, %1 offset:2048"
               ::"s"(lds), "s"(sb), "v"(v0), "v"(v1), "v"(v2) : "memory");
}
#define SB() __builtin_amdgcn_sched_barrier(0)
#define PIN(x) asm volatile("" : "+v"(x))
#define PINA(x) asm volatile("" : "+a"(x))
DI unsigned opaque(unsigned v) { asm volatile("" : "+v"(v)); return v & 0x3ffffu; }
template <int N> DI void wait_vm() { asm volatile("s_waitcnt vmcnt(%0)" ::"n"(N) : "memory"); }
DI void wait_lgkm0() { asm volatile("s_waitcnt lgkmcnt(0)" ::: "memory"); }
DI void bar() { asm volatile("s_waitcnt lgkmcnt(0)" ::: "memory"); __builtin_amdgcn_s_barrier(); asm volatile("" ::: "memory"); }
DI v4u ld_sc1(__amdgpu_buffer_rsrc_t rs, unsigned voff, unsigned soff) { return __builtin_amdgcn_raw_buffer_load_b128(rs, voff, soff, AUX_SC1); }
DI void st_sc1(v4u v, __amdgpu_buffer_rsrc_t rs, unsigned voff, unsigned soff) { __builtin_amdgcn_raw_buffer_store_b128(v, rs, voff, soff, AUX_SC1); }

DI void publish(char* ws, unsigned off, u64 tag, int tid) {
  wait_vm<0>();
  bar();
  if ((tid >> 3) == 56) __hip_atomic_store((gu64*)(ws + off + (tid & 7) * REPB), tag, __ATOMIC_RELAXED, __HIP_MEMORY_SCOPE_AGENT);
}
DI void poll(char* ws, unsigned off, int lo, int hi, u64 tag, int lane) {
  int i = lo + lane; i = i < hi ? i : hi - 1;
  gu64* p = (gu64*)(ws + off) + i;
#pragma unroll 1
  for (unsigned spins = 0; spins < SPIN_CAP; ++spins) {
    u64 v = __hip_atomic_load(p, __ATOMIC_RELAXED, __HIP_MEMORY_SCOPE_AGENT);
    if (__all(v == tag)) break;
    __builtin_amdgcn_s_sleep(2);
  }
  __builtin_amdgcn_fence(__ATOMIC_ACQUIRE, "wavefront");
  asm volatile("" ::: "memory");
}

template <int CTRL> DI float dpp_add(float v) {
  return v + __uint_as_float(__builtin_amdgcn_update_dpp(0u, __float_as_uint(v), CTRL, 0xf, 0xf, true));
}
DI float x16_add(float v) { auto r = __builtin_amdgcn_permlane16_swap(__float_as_uint(v), __float_as_uint(v), false, false); return __uint_as_float(r[0]) + __uint_as_float(r[1]); }
DI float x32_add(float v) { auto r = __builtin_amdgcn_permlane32_swap(__float_as_uint(v), __float_as_uint(v), false, false); return __uint_as_float(r[0]) + __uint_as_float(r[1]); }
DI float wave_sum(float v) {
  v = dpp_add<0xB1>(v);
  v = dpp_add<0x4E>(v);
  v = dpp_add<0x141>(v);
  v = dpp_add<0x140>(v);
  return x32_add(x16_add(v));
}
DI void x32_max2(float& a, float& b) {
  auto r = __builtin_amdgcn_permlane32_swap(__float_as_uint(a), __float_as_uint(b), false, false);
  float m = fmaxf(__uint_as_float(r[0]), __uint_as_float(r[1]));
  auto q = __builtin_amdgcn_permlane32_swap(__float_as_uint(m), __float_as_uint(m), false, false);
  a = __uint_as_float(q[0]); b = __uint_as_float(q[1]);
}
DI void x32_add2(float& a, float& b) {
  auto r = __builtin_amdgcn_permlane32_swap(__float_as_uint(a), __float_as_uint(b), false, false);
  float m = __uint_as_float(r[0]) + __uint_as_float(r[1]);
  auto q = __builtin_amdgcn_permlane32_swap(__float_as_uint(m), __float_as_uint(m), false, false);
  a = __uint_as_float(q[0]); b = __uint_as_float(q[1]);
}
template <int O> DI void hedgehog(const f32x16& P, float c, v4u& fp, v4u& fn) {
  float mx = fmaxf(fmaxf(fmaxf(P[O], P[O + 1]), fmaxf(P[O + 2], P[O + 3])), fmaxf(fmaxf(P[O + 4], P[O + 5]), fmaxf(P[O + 6], P[O + 7])));
  float mn = -fminf(fminf(fminf(P[O], P[O + 1]), fminf(P[O + 2], P[O + 3])), fminf(fminf(P[O + 4], P[O + 5]), fminf(P[O + 6], P[O + 7])));
  x32_max2(mx, mn);
  const float bp = -mx * c, bn = -mn * c;
  float ep[8], en[8];
#pragma unroll
  for (int j = 0; j < 8; ++j) {
    ep[j] = __builtin_amdgcn_exp2f(__builtin_fmaf(P[O + j], c, bp));
    en[j] = __builtin_amdgcn_exp2f(__builtin_fmaf(P[O + j], -c, bn));
  }
  float sp = ((ep[0] + ep[1]) + (ep[2] + ep[3])) + ((ep[4] + ep[5]) + (ep[6] + ep[7]));
  float sn = ((en[0] + en[1]) + (en[2] + en[3])) + ((en[4] + en[5]) + (en[6] + en[7]));
  x32_add2(sp, sn);
  const float rp = __builtin_amdgcn_rcpf(sp), rn = __builtin_amdgcn_rcpf(sn);
  fp = v4u{pk(ep[0] * rp, ep[1] * rp), pk(ep[2] * rp, ep[3] * rp), pk(ep[4] * rp, ep[5] * rp), pk(ep[6] * rp, ep[7] * rp)};
  fn = v4u{pk(en[0] * rn, en[1] * rn), pk(en[2] * rn, en[3] * rn), pk(en[4] * rn, en[5] * rn), pk(en[6] * rn, en[7] * rn)};
}
DI void split3(float v, unsigned& a, unsigned& b, unsigned& c) {
  a = pk(v, 0.f) & 0xffffu; float r = v - bflo(a);
  b = pk(r, 0.f) & 0xffffu; r -= bflo(b);
  c = pk(r, 0.f) & 0xffffu;
}
DI float gelu(float x) {
  const float y = x * __builtin_fmaf(x * x, 0.044715f * 0.7978845608028654f, 0.7978845608028654f);
  return x * __builtin_amdgcn_rcpf(1.f + __builtin_amdgcn_exp2f(-2.f * LOG2E * y));
}
DI u64 ld_epoch(const char* ws) {
  u64 e;
  asm volatile("s_load_dwordx2 %0, %1, 0x0\n\ts_waitcnt lgkmcnt(0)" : "=s"(e) : "s"(ws) : "memory");
  return e;
}
DI u64 mix64(u64 z) {
  z += 0x9E3779B97F4A7C15ull; z = (z ^ (z >> 30)) * 0xBF58476D1CE4E5B9ull; z = (z ^ (z >> 27)) * 0x94D049BB133111EBull;
  return z ^ (z >> 31);
}

#define CYC_BEGIN(v)
#define CYC_END(v, acc_)
#define TS(k) do { } while (0)

constexpr int DH = 12;
constexpr int DU = 14;
constexpr int b_glds(int i) { return ((i & 3) == 3 && i / 4 <= 4) || ((i & 3) == 0 && i >= 4 && i / 4 <= 5) ? 3 : 0; }
constexpr int b_hld(int i) { return (i + DH < 32) ? 1 : 0; }
constexpr int b_wait(int s) {
  int n = b_hld(4 * (s - 1));
  for (int i = 4 * (s - 1) + 1; i < 4 * s + 3; ++i) n += b_glds(i) + b_hld(i) + (i >= 24 ? 1 : 0);
  return n;
}

struct P3Src { const char* wo; const char* w2; unsigned vo, v2; int nu; unsigned b0, b1, b2; };
DI unsigned p3_row(const P3Src& s, int i) {
  const int j = i < 20 ? i : i - 20;
  return j < 8 ? s.b0 + j * ROW : j < 15 ? s.b1 + (j - 8) * ROW : s.b2 + (j - 15) * ROW;
}
DI void p3_issue(const P3Src& s, int i, int w) {
  const unsigned lds = __builtin_amdgcn_readfirstlane(p3_row(s, i) + w * 1024);
  if (i < 8) glds1(lds, s.wo + i * 16 * 1024, s.vo);
  else { int k = i - 8; k = k < s.nu ? k : s.nu - 1; glds1(lds, s.w2 + (size_t)k * 16 * 4096, s.v2); }
}
DI void unpack_add(const v4u f, float& s) {
  s += ((bflo(f.x) + bfhi(f.x)) + (bflo(f.y) + bfhi(f.y))) + ((bflo(f.z) + bfhi(f.z)) + (bflo(f.w) + bfhi(f.w)));
}
DI v4u f4(float a, float b, float c, float d) { return v4u{__float_as_uint(a), __float_as_uint(b), __float_as_uint(c), __float_as_uint(d)}; }

__global__ __launch_bounds__(512, 2) void fused(Params p) {
  extern __shared__ __attribute__((aligned(16))) char smem[];
  const int tid = threadIdx.x, lane = tid & 63, w = __builtin_amdgcn_readfirstlane(tid >> 6);
  const int r = lane & 31, hh = lane >> 5, blk = (lane >> 4) & 1, q = (lane >> 2) & 3, pp = lane & 3;
  const int b = blockIdx.x, xg = b & 7, idx = b >> 3, sx = xg >> 1, gx = xg & 1;
  const int wh = w >> 1, wl = w & 1;
  TS(0);
  u64 tag;
  const __amdgpu_buffer_rsrc_t rs = __builtin_amdgcn_make_buffer_rsrc(p.ws, 0, WS_BYTES, 0x00020000);
  const unsigned rep = xg * REPB;
  asm volatile("" ::"v"((LAS char*)smem));
  f32x16 zero;
#pragma unroll
  for (int e = 0; e < 16; ++e) zero[e] = 0.f;

  const int i16 = idx & 15;
  const int st64 = 8 * sx + (i16 & 7);
  const int dt3 = 4 * gx + (((i16 >> 3) + 2 * (i16 & 1) + 2 * (idx >> 4)) & 3);
  const int ks0 = wh < 3 ? 22 * wh : 65;
  P3Src s3;
  {
    const unsigned row = 8 * wl + (lane >> 3), ch = ((lane & 7) ^ (((lane >> 4) & 1) << 2)) * 16;
    s3.wo = p.wo + (size_t)(128 * wh) * 1024 + dt3 * 128;
    s3.w2 = p.w2 + (size_t)ks0 * 16 * 4096 + st64 * 128;
    s3.vo = row * 1024 + ch;
    s3.v2 = row * 4096 + ch;
    s3.nu = wh < 2 ? 22 : 21;
    s3.b0 = idx < 16 ? 0 : 12 * ROW; s3.b1 = idx < 16 ? 8 * ROW : 0; s3.b2 = idx < 16 ? 15 * ROW : 7 * ROW;
  }
  int p3_have;

  if (idx < 16) {
    const int st = 4 * sx + (idx & 3), g = 4 * gx + (idx >> 2), rg = wh, cg = wl, pair = 2 * g + cg;
    const int rot = 2 * (((idx & 3) + (idx >> 2)) & 3);
    const unsigned xc = ((lane & 7) ^ (lane >> 4)) * 16, xr = (lane >> 3) * 1024;
    const unsigned vx0 = xr + xc, vx1 = xr + (xc ^ 64) + 8192 - 1024;
    const unsigned vw0 = (lane >> 3) * 3072 + ((lane & 7) ^ (((lane >> 4) & 1) << 2)) * 16;
    const char* xs = p.x + (size_t)(128 * st + 16 * w) * 1024;
    const char* wsrc = p.wqkv + (size_t)(8 * w) * 3072 + g * 128;
    auto issue_x = [&](int c) { glds2((c & 3) * REG + w * 2048, xs + ((c + rot) & 7) * 128, vx0, vx1); };
    auto issue_w = [&](int c, int t) { glds1((c & 3) * REG + 16384 + t * 8192 + w * 1024, wsrc + (size_t)((c + rot) & 7) * 64 * 3072 + t * 1024, vw0); };
    unsigned fp8[8];
    {
      const unsigned vo = ((2 * pair + (r >> 4)) * 256 + (r & 15) + 64 * hh) * 2;
      asm volatile("global_load_ushort %0, %8, %9\n\tglobal_load_ushort %1, %8, %9 offset:32\n\t"
                   "global_load_ushort %2, %8, %9 offset:64\n\tglobal_load_ushort %3, %8, %9 offset:96\n\t"
                   "global_load_ushort %4, %8, %9 offset:256\n\tglobal_load_ushort %5, %8, %9 offset:288\n\t"
                   "global_load_ushort %6, %8, %9 offset:320\n\tglobal_load_ushort %7, %8, %9 offset:352"
                   : "=&v"(fp8[0]), "=&v"(fp8[1]), "=&v"(fp8[2]), "=&v"(fp8[3]), "=&v"(fp8[4]), "=&v"(fp8[5]), "=&v"(fp8[6]), "=&v"(fp8[7])
                   : "v"(vo), "s"(p.wphi) : "memory");
    }
#pragma unroll
    for (int c = 0; c < 4; ++c) { issue_x(c); issue_w(c, 0); issue_w(c, 1); issue_w(c, 2); }
    tag = mix64(ld_epoch(p.ws));
    f32x16 acc[3] = {zero, zero, zero};
    float sum1 = 0.f, sq1 = 0.f, cs[3] = {0.f, 0.f, 0.f};
    const int pi = rg ^ cg;
    unsigned xa[4], wa[4];
#pragma unroll
    for (int k = 0; k < 4; ++k) {
      xa[k] = (32 * rg + r) * 128 + (unsigned)(((2 * (k ^ pi) + hh) ^ ((r >> 1) & 7)) << 4);
      wa[k] = 16384 + (16 * (k ^ pi) + 8 * hh + q) * 128 + ((64 * cg + 32 * blk + 8 * pp) ^ ((q >> 1) << 6));
    }
    v4u fx[2], fw[2][3];
    auto rdw = [&](unsigned a, int bf, int t) { fw[bf][t] = trfrag(a + t * 8192, a + t * 8192 + 512); };
    PIN(acc[0]); PIN(acc[1]); PIN(acc[2]);
    wait_vm<15>();
    bar();
    TS(1);
    fx[0] = lds_r128(xa[0]); rdw(wa[0], 0, 0); rdw(wa[0], 0, 1); rdw(wa[0], 0, 2);
#pragma unroll 1
    for (int c = 0; c < 8; ++c) {
#pragma unroll
      for (int k = 0; k < 4; ++k) {
        const int cb = k & 1, nb = cb ^ 1, nk = (k + 1) & 3;
        const unsigned sl = ((k == 3 ? c + 1 : c) & 3) * REG;
        if (k == 3) {
          CYC_BEGIN(ta_);
          if (c < 7) wait_vm<10>();
          CYC_END(ta_, dbg0);
          CYC_BEGIN(tc_);
          bar();
          CYC_END(tc_, dbg2);
        }
        SB();
        acc[0] = mfma(fw[cb][0], fx[cb], acc[0]);
        SB();
        fx[nb] = lds_r128(sl + xa[nk]);
        rdw(sl + wa[nk], nb, 0);
        rdw(sl + wa[nk], nb, 1);
        rdw(sl + wa[nk], nb, 2);
        if (k == 0) { unpack_add(fw[cb][0], cs[0]); PIN(cs[0]); }
        SB();
        acc[1] = mfma(fw[cb][1], fx[cb], acc[1]);
        SB();
        if (k == 3) {
          CYC_BEGIN(tb_);
          if (c < 4) { issue_x(c + 4); issue_w(c + 4, 0); issue_w(c + 4, 1); issue_w(c + 4, 2); }
          else if (c < 7) {
            const int i0 = 5 * (c - 4);
#pragma unroll 1
            for (int i = i0; i < i0 + 5; ++i) p3_issue(s3, i, w);
          }
          CYC_END(tb_, dbg1);
        }
        if (k == 0) { unpack_add(fw[cb][1], cs[1]); PIN(cs[1]); }
        if (k & 1) {
#pragma unroll
          for (int e = 0; e < 4; ++e) {
            const float lo = bflo(fx[cb][e]), hi = bfhi(fx[cb][e]);
            sum1 += lo + hi; sq1 = __builtin_fmaf(lo, lo, __builtin_fmaf(hi, hi, sq1));
          }
          PIN(sum1); PIN(sq1);
        }
        SB();
        acc[2] = mfma(fw[cb][2], fx[cb], acc[2]);
        SB();
        if (k == 0) { unpack_add(fw[cb][2], cs[2]); PIN(cs[2]); }
        SB();
      }
    }
    TS(2);
    asm volatile("" : "+v"(fp8[0]), "+v"(fp8[1]), "+v"(fp8[2]), "+v"(fp8[3]), "+v"(fp8[4]), "+v"(fp8[5]), "+v"(fp8[6]), "+v"(fp8[7]));
    v4u aphi0, aphi1, id0, id1;
    {
      const v4u z4 = {0, 0, 0, 0};
      const v4u fphi = {fp8[0] | (fp8[1] << 16), fp8[2] | (fp8[3] << 16), fp8[4] | (fp8[5] << 16), fp8[6] | (fp8[7] << 16)};
      aphi0 = r < 16 ? fphi : z4; aphi1 = r < 16 ? z4 : fphi;
      const int rr = (r & 15) - 4 * hh;
      v4u e1;
      e1.x = rr == 0 ? 0x3f80u : rr == 1 ? 0x3f800000u : 0u; e1.y = rr == 2 ? 0x3f80u : rr == 3 ? 0x3f800000u : 0u;
      e1.z = rr == 8 ? 0x3f80u : rr == 9 ? 0x3f800000u : 0u; e1.w = rr == 10 ? 0x3f80u : rr == 11 ? 0x3f800000u : 0u;
      id0 = r < 16 ? e1 : z4; id1 = r < 16 ? z4 : e1;
    }
    const unsigned EXW = 5120, EX = 3 * REG;
    x32_add2(sum1, sq1);
    cs[0] = x32_add(cs[0]); cs[1] = x32_add(cs[1]); cs[2] = x32_add(cs[2]);
    lds_w64(EX + w * EXW + 4352 + r * 8, v2u{__float_as_uint(sum1), __float_as_uint(sq1)});
    lds_w128(EX + w * EXW + 4608 + r * 16, f4(cs[0], cs[1], cs[2], 0.f));
    bar();
    float rstd;
    {
      const v2u so = lds_r64(EX + (w ^ 1) * EXW + 4352 + r * 8);
      v4u co[4];
#pragma unroll
      for (int i = 0; i < 4; ++i) co[i] = lds_r128(EX + (2 * i + cg) * EXW + 4608 + r * 16);
      const float mean = (sum1 + __uint_as_float(so.x)) * (1.f / 512.f);
      rstd = __builtin_amdgcn_rsqf(fmaxf((sq1 + __uint_as_float(so.y)) * (1.f / 512.f) - mean * mean, 0.f) + 1e-5f);
      unsigned a, bb, c;
      split3(-mean, a, bb, c);
      const v4u mf = hh ? v4u{0, 0, 0, 0} : v4u{a | (bb << 16), a | (c << 16), bb | (a << 16), 0u};
#pragma unroll
      for (int t = 2; t >= 0; --t) {
        split3((__uint_as_float(co[0][t]) + __uint_as_float(co[1][t])) + (__uint_as_float(co[2][t]) + __uint_as_float(co[3][t])), a, bb, c);
        const v4u cf = hh ? v4u{0, 0, 0, 0} : v4u{a | (a << 16), bb | (a << 16), bb | (c << 16), 0u};
        acc[t] = mfma(cf, mf, acc[t]);
      }
    }
    const float cexp = rstd * LOG2E;
    {
      f32x16 P = mfma(aphi0, pack8<0>(acc[1]), zero);
      P = mfma(aphi1, pack8<8>(acc[1]), P);
      f32x16 vs;
#pragma unroll
      for (int e = 0; e < 16; ++e) vs[e] = acc[2][e] * rstd;
      f32x16 zv = mfma(pack8<0>(vs), id0, zero);
      zv = mfma(pack8<8>(vs), id1, zv);
      const v4u vt0 = pack8<0>(zv), vt1 = pack8<8>(zv);
      v4u fp, fn;
      float z0 = 0.f, z1 = 0.f;
      hedgehog<0>(P, cexp, fp, fn);
      f32x16 zf = mfma(fp, id0, zero); zf = mfma(fn, id1, zf);
      hedgehog<8>(P, cexp, fp, fn);
      f32x16 zg = mfma(fp, id0, zero); zg = mfma(fn, id1, zg);
      f32x16 kv0 = mfma(pack8<0>(zf), vt0, zero); kv0 = mfma(pack8<8>(zf), vt1, kv0);
      f32x16 kv1 = mfma(pack8<0>(zg), vt0, zero); kv1 = mfma(pack8<8>(zg), vt1, kv1);
#pragma unroll
      for (int e = 0; e < 16; e += 2) { z0 += zf[e] + zf[e + 1]; z1 += zg[e] + zg[e + 1]; }
      auto zz = __builtin_amdgcn_permlane32_swap(__float_as_uint(z0), __float_as_uint(z1), false, false);
      lds_w32(EX + w * EXW + 4096 + lane * 4, __uint_as_float(zz[0]) + __uint_as_float(zz[1]));
#pragma unroll
      for (int e = 0; e < 16; ++e) kv0[e] = r < 16 ? kv0[e] : kv1[e];
#pragma unroll
      for (int i = 0; i < 4; ++i) lds_w128(EX + w * EXW + i * 1024 + lane * 16, f4(kv0[4 * i], kv0[4 * i + 1], kv0[4 * i + 2], kv0[4 * i + 3]));
      bar();
      if (rg < 2) {
        float s[8];
#pragma unroll
        for (int i = 0; i < 4; ++i) {
          const unsigned a = EX + (2 * i + cg) * EXW + rg * 2048 + lane * 16;
          const v4u lo = lds_r128(a), hi = lds_r128(a + 1024);
#pragma unroll
          for (int e = 0; e < 4; ++e) {
            s[e] = i ? s[e] + __uint_as_float(lo[e]) : __uint_as_float(lo[e]);
            s[4 + e] = i ? s[4 + e] + __uint_as_float(hi[e]) : __uint_as_float(hi[e]);
          }
        }
        st_sc1(v4u{pk(s[0], s[1]), pk(s[2], s[3]), pk(s[4], s[5]), pk(s[6], s[7])}, rs, lane * 16, WS_KVP + ((pair * 16 + st) * 2 + rg) * 1024);
      } else if (rg == 2) {
        const unsigned a = EX + cg * EXW + 4096 + lane * 4;
        const float zt = (lds_r32(a) + lds_r32(a + 2 * EXW)) + (lds_r32(a + 4 * EXW) + lds_r32(a + 6 * EXW));
        __builtin_amdgcn_raw_buffer_store_b32(__float_as_uint(zt), rs, lane * 4, WS_ZP + (pair * 16 + st) * 256, AUX_SC1);
      }
    }
    publish(p.ws, WS_KF + (g * 16 + st) * 8, tag, tid);
    TS(3);
    v4u fq[2][2];
    {
      f32x16 P = mfma(aphi0, pack8<0>(acc[0]), zero);
      P = mfma(aphi1, pack8<8>(acc[0]), P);
      hedgehog<0>(P, cexp, fq[0][0], fq[0][1]);
      hedgehog<8>(P, cexp, fq[1][0], fq[1][1]);
      PIN(fq[0][0]); PIN(fq[0][1]); PIN(fq[1][0]); PIN(fq[1][1]);
    }
    TS(4);
    if (w == 7) poll(p.ws, WS_KF + rep, g * 16, g * 16 + 16, tag, lane);
    bar();
    TS(5);
    {
      v4u kp[8];
#pragma unroll
      for (int i = 0; i < 8; ++i) kp[i] = ld_sc1(rs, lane * 16, WS_KVP + (pair * 32 + (rg & 1) + 2 * (8 * (rg >> 1) + i)) * 1024);
      if (rg == 0) {
        v4u zp[4];
#pragma unroll
        for (int i = 0; i < 4; ++i) zp[i] = ld_sc1(rs, lane * 16, WS_ZP + pair * 4096 + i * 1024);
        float zs[4];
#pragma unroll
        for (int e = 0; e < 4; ++e)
          zs[e] = x32_add(x16_add((__uint_as_float(zp[0][e]) + __uint_as_float(zp[1][e])) + (__uint_as_float(zp[2][e]) + __uint_as_float(zp[3][e]))));
        lds_w64(EX + w * EXW + 4096 + (lane & 15) * 8, v2u{pk(zs[0], zs[1]), pk(zs[2], zs[3])});
      }
      f32x16 Ssum = zero;
#pragma unroll
      for (int i = 0; i < 8; ++i) Ssum = mfma(id0, kp[i], Ssum);
      lds_w128(EX + w * EXW + lane * 16, f4(Ssum[0], Ssum[1], Ssum[2], Ssum[3]));
      lds_w128(EX + w * EXW + 1024 + lane * 16, f4(Ssum[4], Ssum[5], Ssum[6], Ssum[7]));
      bar();
      v4u ah[2][2];
#pragma unroll
      for (int t = 0; t < 2; ++t) {
        const unsigned a = EX + (2 * t + cg) * EXW + lane * 16;
        const v4u a0 = lds_r128(a), a1 = lds_r128(a + 1024), b0 = lds_r128(a + 4 * EXW), b1 = lds_r128(a + 4 * EXW + 1024);
        float s[8];
#pragma unroll
        for (int e = 0; e < 4; ++e) { s[e] = __uint_as_float(a0[e]) + __uint_as_float(b0[e]); s[4 + e] = __uint_as_float(a1[e]) + __uint_as_float(b1[e]); }
        const v4u m = {pk(s[0], s[1]), pk(s[2], s[3]), pk(s[4], s[5]), pk(s[6], s[7])};
#pragma unroll
        for (int h = 0; h < 2; ++h) {
          const unsigned za = EX + cg * EXW + 4096 + (32 * h + 16 * t + 4 * hh) * 2;
          const v2u lo = lds_r64(za), hi = lds_r64(za + 16);
          const v4u zf = {lo.x, lo.y, hi.x, hi.y};
          ah[h][t] = ((r < 16) == (h == 0)) ? m : zf;
        }
      }
      const unsigned ob = WS_O + (((2 * st + (rg >> 1)) * 32 + 2 * pair) * 2 + (rg & 1)) * 1024;
      f32x16 N0 = mfma(ah[0][0], fq[0][0], zero); N0 = mfma(ah[0][1], fq[0][1], N0);
      f32x16 N1 = mfma(ah[1][0], fq[1][0], zero); N1 = mfma(ah[1][1], fq[1][1], N1);
      const float r0 = __builtin_amdgcn_rcpf(N0[8] + 1e-6f), r1 = __builtin_amdgcn_rcpf(N1[0] + 1e-6f);
#pragma unroll
      for (int e = 0; e < 8; ++e) { N0[e] *= r0; N1[8 + e] *= r1; }
      st_sc1(pack8<0>(N0), rs, lane * 16, ob);
      st_sc1(pack8<8>(N1), rs, lane * 16, ob + 2048);
    }
    publish(p.ws, WS_OF + (st * 8 + g) * 8, tag, tid);
    TS(6);
    p3_have = 15;
  } else {
    const int jb = 32 * sx + 16 * gx + (idx - 16);
    const bool gemm2 = idx != 31;
    const int n2 = 15 * sx + (idx - 16), ft = n2 >> 2, dt = 4 * gx + (n2 & 3);
    const int f0 = ft < 14 ? 96 * ft : 1280;
    const int rot = 2 * ((dt + ft) & 3);
    unsigned vw[3];
#pragma unroll
    for (int n = 0; n < 3; ++n) { const unsigned id = 64 * n + lane, row = id / 12, ch = id % 12; vw[n] = row * 2752 + ch * 16 - 1024 * n; }
    const char* w1s = p.w1 + f0 * 2;
    auto issue = [&](int s, int kq, int k) {
      glds3(__builtin_amdgcn_readfirstlane((s % 3) * STG + kq * 12288 + k * 3072), w1s + (size_t)((512 * kq + 64 * ((s + rot) & 7) + 16 * k) * 2752), vw[0], vw[1], vw[2]);
    };
    if (w >= 6) {
      const unsigned sg = 3 * STG + wl * 8192;
      const char* xs = p.x + (size_t)(16 * jb + 8 * wl) * 1024;
      v4u xr[8];
#pragma unroll
      for (int i = 0; i < 8; ++i) xr[i] = *(const v4u*)(xs + i * 1024 + lane * 16);
      tag = mix64(ld_epoch(p.ws));
      float v[8][8], m[8], sc[8];
#pragma unroll
      for (int i = 0; i < 8; ++i) {
#pragma unroll
        for (int e = 0; e < 4; ++e) { v[i][2 * e] = bflo(xr[i][e]); v[i][2 * e + 1] = bfhi(xr[i][e]); }
        m[i] = ((v[i][0] + v[i][1]) + (v[i][2] + v[i][3])) + ((v[i][4] + v[i][5]) + (v[i][6] + v[i][7]));
      }
      TS(1);
#pragma unroll
      for (int i = 0; i < 8; ++i) m[i] = wave_sum(m[i]) * (1.f / 512.f);
#pragma unroll
      for (int i = 0; i < 8; ++i) {
        float s = 0.f;
#pragma unroll
        for (int e = 0; e < 8; ++e) { v[i][e] -= m[i]; s = __builtin_fmaf(v[i][e], v[i][e], s); }
        sc[i] = s;
      }
#pragma unroll
      for (int i = 0; i < 8; ++i) {
        const float var = wave_sum(sc[i]) * (1.f / 512.f);
        const float r1 = __builtin_amdgcn_rsqf(var + 1e-5f);
        sc[i] = r1 * __builtin_amdgcn_rsqf(var * r1 * r1 + 1e-5f);
      }
#pragma unroll
      for (int i = 0; i < 8; ++i)
        lds_w128(sg + i * 1024 + ((lane * 16) ^ ((i & 3) << 6)),
                 v4u{pk(v[i][0] * sc[i], v[i][1] * sc[i]), pk(v[i][2] * sc[i], v[i][3] * sc[i]), pk(v[i][4] * sc[i], v[i][5] * sc[i]), pk(v[i][6] * sc[i], v[i][7] * sc[i])});
      wait_lgkm0();
#pragma unroll
      for (int i = 0; i < 8; ++i) {
        const unsigned a = sg + q * 1024 + ((128 * i + 64 * hh + 32 * blk + 8 * pp) ^ (q << 6));
        const v4u f = trfrag(a, a + 4096);
        st_sc1(f, rs, hh * 1024 + wl * 512 + r * 16, WS_H + (i * 128 + jb) * 2048);
      }
      wait_vm<0>();
      if (lane < 8) __hip_atomic_store((gu64*)(p.ws + WS_HF + (2 * jb + wl) * 8 + lane * REPB), tag, __ATOMIC_RELAXED, __HIP_MEMORY_SCOPE_AGENT);
      TS(2);
    } else {
      if (gemm2) {
#pragma unroll 1
        for (int u = w; u < 48; u += 6) issue(u >> 4, (u >> 2) & 3, u & 3);
      }
      tag = 0;
    }
    if (gemm2) {
      const unsigned tb = (8 * hh + q) * 192 + 32 * blk + 8 * pp + wh * 12288;
      const unsigned ta[3] = {opaque(tb), opaque(tb + STG), opaque(tb + 2 * STG)};
      v4u wr[33][3], hr[32];
      auto rd = [&](int i, int fb) {
        const unsigned a = ta[(i >> 2) % 3] + (i & 3) * 3072 + 64 * fb;
        wr[i][fb] = trfrag(a, a + 768);
      };
      auto ldh = [&](int i) {
        hr[i] = ld_sc1(rs, lane * 16 + (i & 1) * 2048, WS_H + (dt * 128 + 32 * wh + 4 * (((i >> 2) + rot) & 7) + (i & 2)) * 2048 + wl * 1024);
      };
      const int nht = w < 4 ? 2 : 1, ht0 = w < 4 ? 2 * w : 4 + w;
      v2u bb[2][2];
#pragma unroll
      for (int e = 0; e < 2; ++e) {
        const char* bp = p.b1 + (f0 + 16 * ((ht0 + (e < nht ? e : 0)) >> 1) + 4 * hh) * 2;
        bb[e][0] = *(const v2u*)bp; bb[e][1] = *(const v2u*)(bp + 16);
      }
      if (w == 7) {
#pragma unroll 1
        for (unsigned spins = 0; spins < SPIN_CAP; ++spins) {
          const v4u a = ld_sc1(rs, lane * 16, WS_HF + rep), c = ld_sc1(rs, lane * 16 + 1024, WS_HF + rep);
          const unsigned tl = (unsigned)tag, th = (unsigned)(tag >> 32);
          const bool ok = a.x == tl && a.y == th && a.z == tl && a.w == th && c.x == tl && c.y == th && c.z == tl && c.w == th;
          if (__all(ok)) break;
          __builtin_amdgcn_s_sleep(2);
          asm volatile("" ::: "memory");
        }
      }
      wait_vm<0>();
      bar();
      TS(3);
#pragma unroll
      for (int i = 0; i < DH; ++i) ldh(i);
      rd(0, 0); rd(0, 1); rd(0, 2);
      SB();
      f32x16 acc[3] = {zero, zero, zero};
#pragma unroll
      for (int i = 0; i < 32; ++i) {
        const int s = i >> 2, k = i & 3;
        if (k == 3) {
          CYC_BEGIN(ta_);
          switch (s) {
#define BW(n) case n: wait_vm<b_wait(n)>(); break;
            BW(2) BW(3) BW(4) BW(5) BW(6)
#undef BW
          }
          CYC_END(ta_, dbg0);
          CYC_BEGIN(tc_);
          bar();
          CYC_END(tc_, dbg2);
        }
        SB();
        acc[0] = mfma(wr[i][0], hr[i], acc[0]);
        SB();
        if (i + 1 < 32) { rd(i + 1, 0); rd(i + 1, 1); rd(i + 1, 2); }
        SB();
        acc[1] = mfma(wr[i][1], hr[i], acc[1]);
        SB();
        if (b_glds(i)) { CYC_BEGIN(tb_); if (k == 3) issue(s + 3, wh, 2 * wl); else issue(s + 2, wh, 2 * wl + 1); CYC_END(tb_, dbg1); }
        if (i == 24) { PIN(hr[31]); }
        if (i >= 24) p3_issue(s3, i - 24, w);
        SB();
        acc[2] = mfma(wr[i][2], hr[i], acc[2]);
        SB();
        if (b_hld(i)) ldh(i + DH);
        SB();
      }
      TS(4);
#pragma unroll
      for (int fb = 0; fb < 3; ++fb)
#pragma unroll
        for (int qd = 0; qd < 4; ++qd)
          lds_w128(w * 12288 + (fb * 4 + qd) * 1024 + lane * 16, f4(acc[fb][4 * qd], acc[fb][4 * qd + 1], acc[fb][4 * qd + 2], acc[fb][4 * qd + 3]));
      bar();
#pragma unroll
      for (int e = 0; e < 2; ++e) {
        if (e < nht) {
          const int ht = ht0 + e;
          const unsigned a = (ht & 1) * 12288 + (ht >> 1) * 2048 + lane * 16;
          float s[8] = {bflo(bb[e][0].x), bfhi(bb[e][0].x), bflo(bb[e][0].y), bfhi(bb[e][0].y), bflo(bb[e][1].x), bfhi(bb[e][1].x), bflo(bb[e][1].y), bfhi(bb[e][1].y)};
#pragma unroll
          for (int ww = 0; ww < 4; ++ww) {
            const v4u lo = lds_r128(a + ww * 24576), hi = lds_r128(a + ww * 24576 + 1024);
#pragma unroll
            for (int j = 0; j < 4; ++j) { s[j] += __uint_as_float(lo[j]); s[4 + j] += __uint_as_float(hi[j]); }
          }
          const v4u uf = {pk(gelu(s[0]), gelu(s[1])), pk(gelu(s[2]), gelu(s[3])), pk(gelu(s[4]), gelu(s[5])), pk(gelu(s[6]), gelu(s[7]))};
          if (ft < 14 || ht >= 8) st_sc1(uf, rs, lane * 16, WS_U + ((dt * 86 + (f0 >> 4) + (ht >> 1)) * 2 + (ht & 1)) * 1024);
        }
      }
      publish(p.ws, WS_UF + (dt * 16 + ft) * 8, tag, tid);
      TS(6);
    } else {
      bar();
#pragma unroll 1
      for (int i = 0; i < 8; ++i) p3_issue(s3, i, w);
      wait_vm<0>();
      bar();
    }
    p3_have = 8;
  }

  {
#pragma unroll 1
    for (int i = p3_have; i < 20; ++i) p3_issue(s3, i, w);
    const int srow = 64 * st64 + (tid >> 3);
    const v4u xres = *(const v4u*)(p.x + (size_t)srow * 1024 + dt3 * 128 + (tid & 7) * 16);
    const float b2v = bflo(*(const unsigned short*)(p.b2 + srow * 2));
    const unsigned tb = wh * 2048 + (4 * hh + q) * 128 + ((32 * blk + 8 * pp) ^ ((q >> 1) << 6));
    const unsigned t0[2] = {opaque(s3.b0 + tb), opaque(s3.b0 + (tb ^ 64))};
    const unsigned t1[2] = {opaque(s3.b1 + tb), opaque(s3.b1 + (tb ^ 64))};
    const unsigned t2[2] = {opaque(s3.b2 + tb), opaque(s3.b2 + (tb ^ 64))};
    const unsigned tw = opaque(s3.b0 + (tb ^ (wl << 6)));
    v4u wof[8], wr[31][2], ar[8][2], ur[22];
    auto rd = [&](int i, int c) {
      const int j = i < 20 ? i : i - 20;
      const unsigned a = j < 8 ? t0[c] + j * ROW : j < 15 ? t1[c] + (j - 8) * ROW : t2[c] + (j - 15) * ROW;
      wr[i][c] = trfrag(a, a + 1024);
    };
#pragma unroll
    for (int i = 0; i < 8; ++i) wof[i] = trfrag(tw + i * ROW, tw + i * ROW + 1024);
    bar();
#pragma unroll
    for (int i = 20; i < 28; ++i) p3_issue(s3, i, w);
    TS(7);
    if (w == 7) poll(p.ws, WS_OF + rep, (st64 >> 1) * 8, (st64 >> 1) * 8 + 8, tag, lane);
    wait_vm<0>();
    bar();
    TS(8);
#pragma unroll
    for (int i = 0; i < 8; ++i) {
      const unsigned so = WS_O + (st64 * 32 + 8 * wh + i) * 2048;
      ar[i][0] = ld_sc1(rs, lane * 16, so); ar[i][1] = ld_sc1(rs, lane * 16 + 1024, so);
    }
    rd(8, 0); rd(8, 1); rd(9, 0); rd(9, 1);
    bar();
    p3_issue(s3, 28, w); p3_issue(s3, 29, w);
    SB();
    f32x16 acc[2] = {zero, zero};
#pragma unroll
    for (int i = 0; i < 8; ++i) {
      acc[0] = mfma(wof[i], ar[i][0], acc[0]);
      acc[1] = mfma(wof[i], ar[i][1], acc[1]);
      SB();
    }
    if (w == 7) poll(p.ws, WS_UF + rep, dt3 * 16, dt3 * 16 + 15, tag, lane);
    wait_vm<0>();
    bar();
    TS(9);
    auto ldu = [&](int k) {
      const int kk = k < s3.nu ? k : s3.nu - 1;
      ur[k] = ld_sc1(rs, lane * 16, WS_U + (dt3 * 86 + ks0 + kk) * 2048 + wl * 1024);
    };
#pragma unroll
    for (int k = 0; k < DU; ++k) ldu(k);
    SB();
#pragma unroll
    for (int k = 0; k < 22; ++k) {
      const int i = k + 8;
      if (k < 21 || wh < 2) {
        acc[0] = mfma(ur[k], wr[i][0], acc[0]);
        SB();
        if (i + 2 < 30) { rd(i + 2, 0); rd(i + 2, 1); }
        SB();
        acc[1] = mfma(ur[k], wr[i][1], acc[1]);
        SB();
        if (k + DU < 22) ldu(k + DU);
        SB();
      }
    }
    TS(10);
    bar();
#pragma unroll
    for (int c = 0; c < 2; ++c)
#pragma unroll
      for (int qd = 0; qd < 4; ++qd)
        lds_w128(wh * 17408 + (32 * c + r) * 272 + (32 * wl + 8 * qd + 4 * hh) * 4, f4(acc[c][4 * qd], acc[c][4 * qd + 1], acc[c][4 * qd + 2], acc[c][4 * qd + 3]));
    bar();
    {
      float s[8];
#pragma unroll
      for (int e = 0; e < 4; ++e) { s[2 * e] = bflo(xres[e]) + b2v; s[2 * e + 1] = bfhi(xres[e]) + b2v; }
#pragma unroll
      for (int ww = 0; ww < 4; ++ww) {
        const unsigned a = ww * 17408 + (tid >> 3) * 272 + (tid & 7) * 32;
        const v4u lo = lds_r128(a), hi = lds_r128(a + 16);
#pragma unroll
        for (int j = 0; j < 4; ++j) { s[j] += __uint_as_float(lo[j]); s[4 + j] += __uint_as_float(hi[j]); }
      }
      *(v4u*)(p.out + (size_t)srow * 1024 + dt3 * 128 + (tid & 7) * 16) = v4u{pk(s[0], s[1]), pk(s[2], s[3]), pk(s[4], s[5]), pk(s[6], s[7])};
    }
    if (b == 0 && tid == 448) *(u64*)p.ws = tag;
  }
}
}

extern "C" void kernel_launch(void* const* d_in, const int* in_sizes, int n_in, void* d_out, int out_size, void* d_ws,
                              size_t ws_size, hipStream_t stream) {
  hk::Params p;
  p.x = (const char*)d_in[0]; p.wqkv = (const char*)d_in[1]; p.wphi = (const char*)d_in[2]; p.wo = (const char*)d_in[3];
  p.w1 = (const char*)d_in[5]; p.b1 = (const char*)d_in[6]; p.w2 = (const char*)d_in[7]; p.b2 = (const char*)d_in[8];
  p.out = (char*)d_out; p.ws = (char*)d_ws;
  (void)hipFuncSetAttribute((const void*)hk::fused, hipFuncAttributeMaxDynamicSharedMemorySize, hk::LDS_BYTES);
  hk::fused<<<256, 512, hk::LDS_BYTES, stream>>>(p);
}
```

```cpp
#define TOUCH
#include <hip/hip_runtime.h>
#include <stdint.h>

#define GAS __attribute__((address_space(1)))
#define LAS __attribute__((address_space(3)))
#define DI __device__ __forceinline__
typedef short bf16x8 __attribute__((ext_vector_type(8)));
typedef short s16x4 __attribute__((ext_vector_type(4)));
typedef __bf16 bf2 __attribute__((ext_vector_type(2)));
typedef float f32x16 __attribute__((ext_vector_type(16)));
typedef float f32x4 __attribute__((ext_vector_type(4)));
typedef unsigned v4u __attribute__((ext_vector_type(4)));
typedef unsigned v2u __attribute__((ext_vector_type(2)));
typedef unsigned long long u64;
typedef GAS u64 gu64;

namespace hk {
constexpr unsigned REPB = 8192;
constexpr unsigned WS_HF = 4096, WS_KF = WS_HF + 2048, WS_OF = WS_KF + 1024, WS_UF = WS_OF + 1024;
constexpr unsigned WS_ZP = 131072, WS_KVP = 196608, WS_TS = 768 * 1024;
constexpr unsigned WS_H = 1u << 20, WS_U = 3u << 20, WS_O = 9u << 19, WS_BYTES = 13u << 19;
constexpr unsigned REG = 40960, ROW = 8192, STG = 49152, LDS_BYTES = 163840;
constexpr unsigned SPIN_CAP = 1u << 14;
constexpr int AUX_SC1 = 16;
constexpr float LOG2E = 1.4426950408889634f;
#define CODE_KB 0

struct Params {
  const char *x, *wqkv, *wphi, *wo, *w1, *b1, *w2, *b2;
  char *out, *ws;
};

DI f32x16 mfma(v4u a, v4u b, f32x16 c) {
  return __builtin_amdgcn_mfma_f32_32x32x16_bf16(__builtin_bit_cast(bf16x8, a), __builtin_bit_cast(bf16x8, b), c, 0, 0, 0);
}
DI unsigned pk(float a, float b) { bf2 v = {(__bf16)a, (__bf16)b}; return __builtin_bit_cast(unsigned, v); }
DI float bflo(unsigned u) { return __uint_as_float(u << 16); }
DI float bfhi(unsigned u) { return __uint_as_float(u & 0xffff0000u); }
template <int O> DI v4u pack8(const f32x16& x) {
  return v4u{pk(x[O], x[O + 1]), pk(x[O + 2], x[O + 3]), pk(x[O + 4], x[O + 5]), pk(x[O + 6], x[O + 7])};
}
DI v2u trr(unsigned a) { return __builtin_bit_cast(v2u, __builtin_amdgcn_ds_read_tr16_b64_v4i16((LAS s16x4*)(size_t)a)); }
DI v4u trfrag(unsigned a0, unsigned a1) { v2u lo = trr(a0), hi = trr(a1); return v4u{lo.x, lo.y, hi.x, hi.y}; }
DI v4u lds_r128(unsigned a) { return *(LAS v4u*)(size_t)a; }
DI v2u lds_r64(unsigned a) { return *(LAS v2u*)(size_t)a; }
DI float lds_r32(unsigned a) { return *(LAS float*)(size_t)a; }
DI void lds_w128(unsigned a, v4u v) { *(LAS v4u*)(size_t)a = v; }
DI void lds_w64(unsigned a, v2u v) { *(LAS v2u*)(size_t)a = v; }
DI void lds_w32(unsigned a, float v) { *(LAS float*)(size_t)a = v; }
DI void glds1(unsigned lds, const char* sb, unsigned v0) {
  asm volatile("s_mov_b32 m0, %0\n\ts_nop 0\n\tglobal_load_lds_dwordx4 %2, %1" ::"s"(lds), "s"(sb), "v"(v0) : "memory");
}
DI void glds2(unsigned lds, const char* sb, unsigned v0, unsigned v1) {
  asm volatile("s_mov_b32 m0, %0\n\ts_nop 0\n\tglobal_load_lds_dwordx4 %2, %1\n\tglobal_load_lds_dwordx4 %3, %1 offset:1024"
               ::"s"(lds), "s"(sb), "v"(v0), "v"(v1) : "memory");
}
DI void glds3(unsigned lds, const char* sb, unsigned v0, unsigned v1, unsigned v2) {
  asm volatile("s_mov_b32 m0, %0\n\ts_nop 0\n\tglobal_load_lds_dwordx4 %2, %1\n\tglobal_load_lds_dwordx4 %3, %1 offset:1024\n\t"
               "global_load_lds_dwordx4 %4, %1 offset:2048"
               ::"s"(lds), "s"(sb), "v"(v0), "v"(v1), "v"(v2) : "memory");
}
#define SB() __builtin_amdgcn_sched_barrier(0)
#define PIN(x) asm volatile("" : "+v"(x))
#define PINA(x) asm volatile("" : "+a"(x))
DI unsigned opaque(unsigned v) { asm volatile("" : "+v"(v)); return v & 0x3ffffu; }
template <int N> DI void wait_vm() { asm volatile("s_waitcnt vmcnt(%0)" ::"n"(N) : "memory"); }
DI void wait_lgkm0() { asm volatile("s_waitcnt lgkmcnt(0)" ::: "memory"); }
DI void bar() { asm volatile("s_waitcnt lgkmcnt(0)" ::: "memory"); __builtin_amdgcn_s_barrier(); asm volatile("" ::: "memory"); }
DI v4u ld_sc1(__amdgpu_buffer_rsrc_t rs, unsigned voff, unsigned soff) { return __builtin_amdgcn_raw_buffer_load_b128(rs, voff, soff, AUX_SC1); }
DI void st_sc1(v4u v, __amdgpu_buffer_rsrc_t rs, unsigned voff, unsigned soff) { __builtin_amdgcn_raw_buffer_store_b128(v, rs, voff, soff, AUX_SC1); }

DI void publish(char* ws, unsigned off, u64 tag, int tid) {
  wait_vm<0>();
  bar();
  if ((tid >> 3) == 56) __hip_atomic_store((gu64*)(ws + off + (tid & 7) * REPB), tag, __ATOMIC_RELAXED, __HIP_MEMORY_SCOPE_AGENT);
}
DI void poll(char* ws, unsigned off, int lo, int hi, u64 tag, int lane) {
  int i = lo + lane; i = i < hi ? i : hi - 1;
  gu64* p = (gu64*)(ws + off) + i;
#pragma unroll 1
  for (unsigned spins = 0; spins < SPIN_CAP; ++spins) {
    u64 v = __hip_atomic_load(p, __ATOMIC_RELAXED, __HIP_MEMORY_SCOPE_AGENT);
    if (__all(v == tag)) break;
    __builtin_amdgcn_s_sleep(2);
  }
  __builtin_amdgcn_fence(__ATOMIC_ACQUIRE, "wavefront");
  asm volatile("" ::: "memory");
}

template <int CTRL> DI float dpp_add(float v) {
  return v + __uint_as_float(__builtin_amdgcn_update_dpp(0u, __float_as_uint(v), CTRL, 0xf, 0xf, true));
}
DI float x16_add(float v) { auto r = __builtin_amdgcn_permlane16_swap(__float_as_uint(v), __float_as_uint(v), false, false); return __uint_as_float(r[0]) + __uint_as_float(r[1]); }
DI float x32_add(float v) { auto r = __builtin_amdgcn_permlane32_swap(__float_as_uint(v), __float_as_uint(v), false, false); return __uint_as_float(r[0]) + __uint_as_float(r[1]); }
DI float wave_sum(float v) {
  v = dpp_add<0xB1>(v);
  v = dpp_add<0x4E>(v);
  v = dpp_add<0x141>(v);
  v = dpp_add<0x140>(v);
  return x32_add(x16_add(v));
}
DI void x32_max2(float& a, float& b) {
  auto r = __builtin_amdgcn_permlane32_swap(__float_as_uint(a), __float_as_uint(b), false, false);
  float m = fmaxf(__uint_as_float(r[0]), __uint_as_float(r[1]));
  auto q = __builtin_amdgcn_permlane32_swap(__float_as_uint(m), __float_as_uint(m), false, false);
  a = __uint_as_float(q[0]); b = __uint_as_float(q[1]);
}
DI void x32_add2(float& a, float& b) {
  auto r = __builtin_amdgcn_permlane32_swap(__float_as_uint(a), __float_as_uint(b), false, false);
  float m = __uint_as_float(r[0]) + __uint_as_float(r[1]);
  auto q = __builtin_amdgcn_permlane32_swap(__float_as_uint(m), __float_as_uint(m), false, false);
  a = __uint_as_float(q[0]); b = __uint_as_float(q[1]);
}
template <int O> DI void hedgehog(const f32x16& P, float c, v4u& fp, v4u& fn) {
  float mx = fmaxf(fmaxf(fmaxf(P[O], P[O + 1]), fmaxf(P[O + 2], P[O + 3])), fmaxf(fmaxf(P[O + 4], P[O + 5]), fmaxf(P[O + 6], P[O + 7])));
  float mn = -fminf(fminf(fminf(P[O], P[O + 1]), fminf(P[O + 2], P[O + 3])), fminf(fminf(P[O + 4], P[O + 5]), fminf(P[O + 6], P[O + 7])));
  x32_max2(mx, mn);
  const float bp = -mx * c, bn = -mn * c;
  float ep[8], en[8];
#pragma unroll
  for (int j = 0; j < 8; ++j) {
    ep[j] = __builtin_amdgcn_exp2f(__builtin_fmaf(P[O + j], c, bp));
    en[j] = __builtin_amdgcn_exp2f(__builtin_fmaf(P[O + j], -c, bn));
  }
  float sp = ((ep[0] + ep[1]) + (ep[2] + ep[3])) + ((ep[4] + ep[5]) + (ep[6] + ep[7]));
  float sn = ((en[0] + en[1]) + (en[2] + en[3])) + ((en[4] + en[5]) + (en[6] + en[7]));
  x32_add2(sp, sn);
  const float rp = __builtin_amdgcn_rcpf(sp), rn = __builtin_amdgcn_rcpf(sn);
  fp = v4u{pk(ep[0] * rp, ep[1] * rp), pk(ep[2] * rp, ep[3] * rp), pk(ep[4] * rp, ep[5] * rp), pk(ep[6] * rp, ep[7] * rp)};
  fn = v4u{pk(en[0] * rn, en[1] * rn), pk(en[2] * rn, en[3] * rn), pk(en[4] * rn, en[5] * rn), pk(en[6] * rn, en[7] * rn)};
}
DI void split3(float v, unsigned& a, unsigned& b, unsigned& c) {
  a = pk(v, 0.f) & 0xffffu; float r = v - bflo(a);
  b = pk(r, 0.f) & 0xffffu; r -= bflo(b);
  c = pk(r, 0.f) & 0xffffu;
}
DI float gelu(float x) {
  const float y = x * __builtin_fmaf(x * x, 0.044715f * 0.7978845608028654f, 0.7978845608028654f);
  return x * __builtin_amdgcn_rcpf(1.f + __builtin_amdgcn_exp2f(-2.f * LOG2E * y));
}
DI u64 ld_epoch(const char* ws) {
  u64 e;
  asm volatile("s_load_dwordx2 %0, %1, 0x0\n\ts_waitcnt lgkmcnt(0)" : "=s"(e) : "s"(ws) : "memory");
  return e;
}
DI u64 mix64(u64 z) {
  z += 0x9E3779B97F4A7C15ull; z = (z ^ (z >> 30)) * 0xBF58476D1CE4E5B9ull; z = (z ^ (z >> 27)) * 0x94D049BB133111EBull;
  return z ^ (z >> 31);
}

#define CYC_BEGIN(v)
#define CYC_END(v, acc_)
#define TS(k) do { } while (0)

constexpr int DH = 12;
constexpr int DU = 14;
constexpr int b_glds(int i) { return ((i & 3) == 3 && i / 4 <= 4) || ((i & 3) == 0 && i >= 4 && i / 4 <= 5) ? 3 : 0; }
constexpr int b_hld(int i) { return (i + DH < 32) ? 1 : 0; }
constexpr int b_wait(int s) {
  int n = b_hld(4 * (s - 1));
  for (int i = 4 * (s - 1) + 1; i < 4 * s + 3; ++i) n += b_glds(i) + b_hld(i) + (i >= 24 ? 1 : 0);
  return n;
}

struct P3Src { const char* wo; const char* w2; unsigned vo, v2; int nu; unsigned b0, b1, b2; };
DI unsigned p3_row(const P3Src& s, int i) {
  const int j = i < 20 ? i : i - 20;
  return j < 8 ? s.b0 + j * ROW : j < 15 ? s.b1 + (j - 8) * ROW : s.b2 + (j - 15) * ROW;
}
DI void p3_issue(const P3Src& s, int i, int w) {
  const unsigned lds = __builtin_amdgcn_readfirstlane(p3_row(s, i) + w * 1024);
  if (i < 8) glds1(lds, s.wo + i * 16 * 1024, s.vo);
  else { int k = i - 8; k = k < s.nu ? k : s.nu - 1; glds1(lds, s.w2 + (size_t)k * 16 * 4096, s.v2); }
}
DI void unpack_add(const v4u f, float& s) {
  s += ((bflo(f.x) + bfhi(f.x)) + (bflo(f.y) + bfhi(f.y))) + ((bflo(f.z) + bfhi(f.z)) + (bflo(f.w) + bfhi(f.w)));
}
DI v4u f4(float a, float b, float c, float d) { return v4u{__float_as_uint(a), __float_as_uint(b), __float_as_uint(c), __float_as_uint(d)}; }

__global__ __launch_bounds__(512, 2) void fused(Params p) {
  extern __shared__ __attribute__((aligned(16))) char smem[];
  const char* pc;
  asm volatile("s_getpc_b64 %0" : "=s"(pc));
  pc = (const char*)((size_t)pc & ~(size_t)255);
  const int tid = threadIdx.x, lane = tid & 63, w = __builtin_amdgcn_readfirstlane(tid >> 6);
  const int r = lane & 31, hh = lane >> 5, blk = (lane >> 4) & 1, q = (lane >> 2) & 3, pp = lane & 3;
  const int b = blockIdx.x, xg = b & 7, idx = b >> 3, sx = xg >> 1, gx = xg & 1;
  const int wh = w >> 1, wl = w & 1;
  TS(0);
  u64 tag;
  const __amdgpu_buffer_rsrc_t rs = __builtin_amdgcn_make_buffer_rsrc(p.ws, 0, WS_BYTES, 0x00020000);
  const unsigned rep = xg * REPB;
  asm volatile("" ::"v"((LAS char*)smem));
  f32x16 zero;
#pragma unroll
  for (int e = 0; e < 16; ++e) zero[e] = 0.f;

  const int i16 = idx & 15;
  const int st64 = 8 * sx + (i16 & 7);
  const int dt3 = 4 * gx + (((i16 >> 3) + 2 * (i16 & 1) + 2 * (idx >> 4)) & 3);
  const int ks0 = wh < 3 ? 22 * wh : 65;
  P3Src s3;
  {
    const unsigned row = 8 * wl + (lane >> 3), ch = ((lane & 7) ^ (((lane >> 4) & 1) << 2)) * 16;
    s3.wo = p.wo + (size_t)(128 * wh) * 1024 + dt3 * 128;
    s3.w2 = p.w2 + (size_t)ks0 * 16 * 4096 + st64 * 128;
    s3.vo = row * 1024 + ch;
    s3.v2 = row * 4096 + ch;
    s3.nu = wh < 2 ? 22 : 21;
    s3.b0 = idx < 16 ? 0 : 12 * ROW; s3.b1 = idx < 16 ? 8 * ROW : 0; s3.b2 = idx < 16 ? 15 * ROW : 7 * ROW;
  }
  unsigned touch = 0, touch2 = 0;
  int p3_have;

  if (idx < 16) {
    const int st = 4 * sx + (idx & 3), g = 4 * gx + (idx >> 2), rg = wh, cg = wl, pair = 2 * g + cg;
    const int rot = 2 * (((idx & 3) + (idx >> 2)) & 3);
    const unsigned xc = ((lane & 7) ^ (lane >> 4)) * 16, xr = (lane >> 3) * 1024;
    const unsigned vx0 = xr + xc, vx1 = xr + (xc ^ 64) + 8192 - 1024;
    const unsigned vw0 = (lane >> 3) * 3072 + ((lane & 7) ^ (((lane >> 4) & 1) << 2)) * 16;
    const char* xs = p.x + (size_t)(128 * st + 16 * w) * 1024;
    const char* wsrc = p.wqkv + (size_t)(8 * w) * 3072 + g * 128;
    auto issue_x = [&](int c) { glds2((c & 3) * REG + w * 2048, xs + ((c + rot) & 7) * 128, vx0, vx1); };
    auto issue_w = [&](int c, int t) { glds1((c & 3) * REG + 16384 + t * 8192 + w * 1024, wsrc + (size_t)((c + rot) & 7) * 64 * 3072 + t * 1024, vw0); };
    unsigned fp8[8];
    {
      const unsigned vo = ((2 * pair + (r >> 4)) * 256 + (r & 15) + 64 * hh) * 2;
      asm volatile("global_load_ushort %0, %8, %9\n\tglobal_load_ushort %1, %8, %9 offset:32\n\t"
                   "global_load_ushort %2, %8, %9 offset:64\n\tglobal_load_ushort %3, %8, %9 offset:96\n\t"
                   "global_load_ushort %4, %8, %9 offset:256\n\tglobal_load_ushort %5, %8, %9 offset:288\n\t"
                   "global_load_ushort %6, %8, %9 offset:320\n\tglobal_load_ushort %7, %8, %9 offset:352"
                   : "=&v"(fp8[0]), "=&v"(fp8[1]), "=&v"(fp8[2]), "=&v"(fp8[3]), "=&v"(fp8[4]), "=&v"(fp8[5]), "=&v"(fp8[6]), "=&v"(fp8[7])
                   : "v"(vo), "s"(p.wphi) : "memory");
    }
#pragma unroll
    for (int c = 0; c < 4; ++c) { issue_x(c); issue_w(c, 0); issue_w(c, 1); issue_w(c, 2); }
    tag = mix64(ld_epoch(p.ws));
    f32x16 acc[3] = {zero, zero, zero};
    float sum1 = 0.f, sq1 = 0.f, cs[3] = {0.f, 0.f, 0.f};
    const int pi = rg ^ cg;
    unsigned xa[4], wa[4];
#pragma unroll
    for (int k = 0; k < 4; ++k) {
      xa[k] = (32 * rg + r) * 128 + (unsigned)(((2 * (k ^ pi) + hh) ^ ((r >> 1) & 7)) << 4);
      wa[k] = 16384 + (16 * (k ^ pi) + 8 * hh + q) * 128 + ((64 * cg + 32 * blk + 8 * pp) ^ ((q >> 1) << 6));
    }
    v4u fx[2], fw[2][3];
    auto rdw = [&](unsigned a, int bf, int t) { fw[bf][t] = trfrag(a + t * 8192, a + t * 8192 + 512); };
    PIN(acc[0]); PIN(acc[1]); PIN(acc[2]);
    wait_vm<15>();
    bar();
    TS(1);
    fx[0] = lds_r128(xa[0]); rdw(wa[0], 0, 0); rdw(wa[0], 0, 1); rdw(wa[0], 0, 2);
#pragma unroll 1
    for (int c = 0; c < 8; ++c) {
#pragma unroll
      for (int k = 0; k < 4; ++k) {
        const int cb = k & 1, nb = cb ^ 1, nk = (k + 1) & 3;
        const unsigned sl = ((k == 3 ? c + 1 : c) & 3) * REG;
        if (k == 3) {
          CYC_BEGIN(ta_);
          if (c < 7) wait_vm<10>();
          CYC_END(ta_, dbg0);
          CYC_BEGIN(tc_);
          bar();
          CYC_END(tc_, dbg2);
        }
        SB();
        acc[0] = mfma(fw[cb][0], fx[cb], acc[0]);
        SB();
        fx[nb] = lds_r128(sl + xa[nk]);
        rdw(sl + wa[nk], nb, 0);
        rdw(sl + wa[nk], nb, 1);
        rdw(sl + wa[nk], nb, 2);
        if (k == 0) { unpack_add(fw[cb][0], cs[0]); PIN(cs[0]); }
        SB();
        acc[1] = mfma(fw[cb][1], fx[cb], acc[1]);
        SB();
        if (k == 3) {
          CYC_BEGIN(tb_);
          if (c < 4) { issue_x(c + 4); issue_w(c + 4, 0); issue_w(c + 4, 1); issue_w(c + 4, 2); }
          else if (c < 7) {
            const int i0 = 5 * (c - 4);
#pragma unroll 1
            for (int i = i0; i < i0 + 5; ++i) p3_issue(s3, i, w);
          }
          CYC_END(tb_, dbg1);
        }
        if (k == 0) { unpack_add(fw[cb][1], cs[1]); PIN(cs[1]); }
        if (k & 1) {
#pragma unroll
          for (int e = 0; e < 4; ++e) {
            const float lo = bflo(fx[cb][e]), hi = bfhi(fx[cb][e]);
            sum1 += lo + hi; sq1 = __builtin_fmaf(lo, lo, __builtin_fmaf(hi, hi, sq1));
          }
          PIN(sum1); PIN(sq1);
        }
        SB();
        acc[2] = mfma(fw[cb][2], fx[cb], acc[2]);
        SB();
        if (k == 0) { unpack_add(fw[cb][2], cs[2]); PIN(cs[2]); }
        SB();
      }
    }
    TS(2);
    asm volatile("" : "+v"(fp8[0]), "+v"(fp8[1]), "+v"(fp8[2]), "+v"(fp8[3]), "+v"(fp8[4]), "+v"(fp8[5]), "+v"(fp8[6]), "+v"(fp8[7]));
    v4u aphi0, aphi1, id0, id1;
    {
      const v4u z4 = {0, 0, 0, 0};
      const v4u fphi = {fp8[0] | (fp8[1] << 16), fp8[2] | (fp8[3] << 16), fp8[4] | (fp8[5] << 16), fp8[6] | (fp8[7] << 16)};
      aphi0 = r < 16 ? fphi : z4; aphi1 = r < 16 ? z4 : fphi;
      const int rr = (r & 15) - 4 * hh;
      v4u e1;
      e1.x = rr == 0 ? 0x3f80u : rr == 1 ? 0x3f800000u : 0u; e1.y = rr == 2 ? 0x3f80u : rr == 3 ? 0x3f800000u : 0u;
      e1.z = rr == 8 ? 0x3f80u : rr == 9 ? 0x3f800000u : 0u; e1.w = rr == 10 ? 0x3f80u : rr == 11 ? 0x3f800000u : 0u;
      id0 = r < 16 ? e1 : z4; id1 = r < 16 ? z4 : e1;
    }
    const unsigned EXW = 5120, EX = 3 * REG;
    x32_add2(sum1, sq1);
    cs[0] = x32_add(cs[0]); cs[1] = x32_add(cs[1]); cs[2] = x32_add(cs[2]);
    lds_w64(EX + w * EXW + 4352 + r * 8, v2u{__float_as_uint(sum1), __float_as_uint(sq1)});
    lds_w128(EX + w * EXW + 4608 + r * 16, f4(cs[0], cs[1], cs[2], 0.f));
    bar();
    float rstd;
    {
      const v2u so = lds_r64(EX + (w ^ 1) * EXW + 4352 + r * 8);
      v4u co[4];
#pragma unroll
      for (int i = 0; i < 4; ++i) co[i] = lds_r128(EX + (2 * i + cg) * EXW + 4608 + r * 16);
      const float mean = (sum1 + __uint_as_float(so.x)) * (1.f / 512.f);
      rstd = __builtin_amdgcn_rsqf(fmaxf((sq1 + __uint_as_float(so.y)) * (1.f / 512.f) - mean * mean, 0.f) + 1e-5f);
      unsigned a, bb, c;
      split3(-mean, a, bb, c);
      const v4u mf = hh ? v4u{0, 0, 0, 0} : v4u{a | (bb << 16), a | (c << 16), bb | (a << 16), 0u};
#pragma unroll
      for (int t = 2; t >= 0; --t) {
        split3((__uint_as_float(co[0][t]) + __uint_as_float(co[1][t])) + (__uint_as_float(co[2][t]) + __uint_as_float(co[3][t])), a, bb, c);
        const v4u cf = hh ? v4u{0, 0, 0, 0} : v4u{a | (a << 16), bb | (a << 16), bb | (c << 16), 0u};
        acc[t] = mfma(cf, mf, acc[t]);
      }
    }
    const float cexp = rstd * LOG2E;
    {
      f32x16 P = mfma(aphi0, pack8<0>(acc[1]), zero);
      P = mfma(aphi1, pack8<8>(acc[1]), P);
      f32x16 vs;
#pragma unroll
      for (int e = 0; e < 16; ++e) vs[e] = acc[2][e] * rstd;
      f32x16 zv = mfma(pack8<0>(vs), id0, zero);
      zv = mfma(pack8<8>(vs), id1, zv);
      const v4u vt0 = pack8<0>(zv), vt1 = pack8<8>(zv);
      v4u fp, fn;
      float z0 = 0.f, z1 = 0.f;
      hedgehog<0>(P, cexp, fp, fn);
      f32x16 zf = mfma(fp, id0, zero); zf = mfma(fn, id1, zf);
      hedgehog<8>(P, cexp, fp, fn);
      f32x16 zg = mfma(fp, id0, zero); zg = mfma(fn, id1, zg);
      f32x16 kv0 = mfma(pack8<0>(zf), vt0, zero); kv0 = mfma(pack8<8>(zf), vt1, kv0);
      f32x16 kv1 = mfma(pack8<0>(zg), vt0, zero); kv1 = mfma(pack8<8>(zg), vt1, kv1);
#pragma unroll
      for (int e = 0; e < 16; e += 2) { z0 += zf[e] + zf[e + 1]; z1 += zg[e] + zg[e + 1]; }
      auto zz = __builtin_amdgcn_permlane32_swap(__float_as_uint(z0), __float_as_uint(z1), false, false);
      lds_w32(EX + w * EXW + 4096 + lane * 4, __uint_as_float(zz[0]) + __uint_as_float(zz[1]));
#pragma unroll
      for (int e = 0; e < 16; ++e) kv0[e] = r < 16 ? kv0[e] : kv1[e];
#pragma unroll
      for (int i = 0; i < 4; ++i) lds_w128(EX + w * EXW + i * 1024 + lane * 16, f4(kv0[4 * i], kv0[4 * i + 1], kv0[4 * i + 2], kv0[4 * i + 3]));
      bar();
      if (rg < 2) {
        float s[8];
#pragma unroll
        for (int i = 0; i < 4; ++i) {
          const unsigned a = EX + (2 * i + cg) * EXW + rg * 2048 + lane * 16;
          const v4u lo = lds_r128(a), hi = lds_r128(a + 1024);
#pragma unroll
          for (int e = 0; e < 4; ++e) {
            s[e] = i ? s[e] + __uint_as_float(lo[e]) : __uint_as_float(lo[e]);
            s[4 + e] = i ? s[4 + e] + __uint_as_float(hi[e]) : __uint_as_float(hi[e]);
          }
        }
        st_sc1(v4u{pk(s[0], s[1]), pk(s[2], s[3]), pk(s[4], s[5]), pk(s[6], s[7])}, rs, lane * 16, WS_KVP + ((pair * 16 + st) * 2 + rg) * 1024);
      } else if (rg == 2) {
        const unsigned a = EX + cg * EXW + 4096 + lane * 4;
        const float zt = (lds_r32(a) + lds_r32(a + 2 * EXW)) + (lds_r32(a + 4 * EXW) + lds_r32(a + 6 * EXW));
        __builtin_amdgcn_raw_buffer_store_b32(__float_as_uint(zt), rs, lane * 4, WS_ZP + (pair * 16 + st) * 256, AUX_SC1);
      }
    }
    publish(p.ws, WS_KF + (g * 16 + st) * 8, tag, tid);
    TS(3);
    v4u fq[2][2];
    {
      f32x16 P = mfma(aphi0, pack8<0>(acc[0]), zero);
      P = mfma(aphi1, pack8<8>(acc[0]), P);
      hedgehog<0>(P, cexp, fq[0][0], fq[0][1]);
      hedgehog<8>(P, cexp, fq[1][0], fq[1][1]);
      PIN(fq[0][0]); PIN(fq[0][1]); PIN(fq[1][0]); PIN(fq[1][1]);
    }
    TS(4);
    if (w == 7) poll(p.ws, WS_KF + rep, g * 16, g * 16 + 16, tag, lane);
    bar();
    TS(5);
    {
      v4u kp[8];
#pragma unroll
      for (int i = 0; i < 8; ++i) kp[i] = ld_sc1(rs, lane * 16, WS_KVP + (pair * 32 + (rg & 1) + 2 * (8 * (rg >> 1) + i)) * 1024);
      if (rg == 0) {
        v4u zp[4];
#pragma unroll
        for (int i = 0; i < 4; ++i) zp[i] = ld_sc1(rs, lane * 16, WS_ZP + pair * 4096 + i * 1024);
        float zs[4];
#pragma unroll
        for (int e = 0; e < 4; ++e)
          zs[e] = x32_add(x16_add((__uint_as_float(zp[0][e]) + __uint_as_float(zp[1][e])) + (__uint_as_float(zp[2][e]) + __uint_as_float(zp[3][e]))));
        lds_w64(EX + w * EXW + 4096 + (lane & 15) * 8, v2u{pk(zs[0], zs[1]), pk(zs[2], zs[3])});
      }
      f32x16 Ssum = zero;
#pragma unroll
      for (int i = 0; i < 8; ++i) Ssum = mfma(id0, kp[i], Ssum);
      lds_w128(EX + w * EXW + lane * 16, f4(Ssum[0], Ssum[1], Ssum[2], Ssum[3]));
      lds_w128(EX + w * EXW + 1024 + lane * 16, f4(Ssum[4], Ssum[5], Ssum[6], Ssum[7]));
      bar();
      v4u ah[2][2];
#pragma unroll
      for (int t = 0; t < 2; ++t) {
        const unsigned a = EX + (2 * t + cg) * EXW + lane * 16;
        const v4u a0 = lds_r128(a), a1 = lds_r128(a + 1024), b0 = lds_r128(a + 4 * EXW), b1 = lds_r128(a + 4 * EXW + 1024);
        float s[8];
#pragma unroll
        for (int e = 0; e < 4; ++e) { s[e] = __uint_as_float(a0[e]) + __uint_as_float(b0[e]); s[4 + e] = __uint_as_float(a1[e]) + __uint_as_float(b1[e]); }
        const v4u m = {pk(s[0], s[1]), pk(s[2], s[3]), pk(s[4], s[5]), pk(s[6], s[7])};
#pragma unroll
        for (int h = 0; h < 2; ++h) {
          const unsigned za = EX + cg * EXW + 4096 + (32 * h + 16 * t + 4 * hh) * 2;
          const v2u lo = lds_r64(za), hi = lds_r64(za + 16);
          const v4u zf = {lo.x, lo.y, hi.x, hi.y};
          ah[h][t] = ((r < 16) == (h == 0)) ? m : zf;
        }
      }
      const unsigned ob = WS_O + (((2 * st + (rg >> 1)) * 32 + 2 * pair) * 2 + (rg & 1)) * 1024;
      f32x16 N0 = mfma(ah[0][0], fq[0][0], zero); N0 = mfma(ah[0][1], fq[0][1], N0);
      f32x16 N1 = mfma(ah[1][0], fq[1][0], zero); N1 = mfma(ah[1][1], fq[1][1], N1);
      const float r0 = __builtin_amdgcn_rcpf(N0[8] + 1e-6f), r1 = __builtin_amdgcn_rcpf(N1[0] + 1e-6f);
#pragma unroll
      for (int e = 0; e < 8; ++e) { N0[e] *= r0; N1[8 + e] *= r1; }
      st_sc1(pack8<0>(N0), rs, lane * 16, ob);
      st_sc1(pack8<8>(N1), rs, lane * 16, ob + 2048);
    }
    publish(p.ws, WS_OF + (st * 8 + g) * 8, tag, tid);
    TS(6);
    p3_have = 15;
  } else {
    const int jb = 32 * sx + 16 * gx + (idx - 16);
    const bool gemm2 = idx != 31;
    const int n2 = 15 * sx + (idx - 16), ft = n2 >> 2, dt = 4 * gx + (n2 & 3);
    const int f0 = ft < 14 ? 96 * ft : 1280;
    const int rot = 2 * ((dt + ft) & 3);
    unsigned vw[3];
#pragma unroll
    for (int n = 0; n < 3; ++n) { const unsigned id = 64 * n + lane, row = id / 12, ch = id % 12; vw[n] = row * 2752 + ch * 16 - 1024 * n; }
    const char* w1s = p.w1 + f0 * 2;
    auto issue = [&](int s, int kq, int k) {
      glds3(__builtin_amdgcn_readfirstlane((s % 3) * STG + kq * 12288 + k * 3072), w1s + (size_t)((512 * kq + 64 * ((s + rot) & 7) + 16 * k) * 2752), vw[0], vw[1], vw[2]);
    };
    if (w >= 6) {
      const unsigned sg = 3 * STG + wl * 8192;
      const char* xs = p.x + (size_t)(16 * jb + 8 * wl) * 1024;
      v4u xr[8];
#pragma unroll
      for (int i = 0; i < 8; ++i) xr[i] = *(const v4u*)(xs + i * 1024 + lane * 16);
      tag = mix64(ld_epoch(p.ws));
      float v[8][8], m[8], sc[8];
#pragma unroll
      for (int i = 0; i < 8; ++i) {
#pragma unroll
        for (int e = 0; e < 4; ++e) { v[i][2 * e] = bflo(xr[i][e]); v[i][2 * e + 1] = bfhi(xr[i][e]); }
        m[i] = ((v[i][0] + v[i][1]) + (v[i][2] + v[i][3])) + ((v[i][4] + v[i][5]) + (v[i][6] + v[i][7]));
      }
      TS(1);
#pragma unroll
      for (int i = 0; i < 8; ++i) m[i] = wave_sum(m[i]) * (1.f / 512.f);
#pragma unroll
      for (int i = 0; i < 8; ++i) {
        float s = 0.f;
#pragma unroll
        for (int e = 0; e < 8; ++e) { v[i][e] -= m[i]; s = __builtin_fmaf(v[i][e], v[i][e], s); }
        sc[i] = s;
      }
#pragma unroll
      for (int i = 0; i < 8; ++i) {
        const float var = wave_sum(sc[i]) * (1.f / 512.f);
        const float r1 = __builtin_amdgcn_rsqf(var + 1e-5f);
        sc[i] = r1 * __builtin_amdgcn_rsqf(var * r1 * r1 + 1e-5f);
      }
#pragma unroll
      for (int i = 0; i < 8; ++i)
        lds_w128(sg + i * 1024 + ((lane * 16) ^ ((i & 3) << 6)),
                 v4u{pk(v[i][0] * sc[i], v[i][1] * sc[i]), pk(v[i][2] * sc[i], v[i][3] * sc[i]), pk(v[i][4] * sc[i], v[i][5] * sc[i]), pk(v[i][6] * sc[i], v[i][7] * sc[i])});
      wait_lgkm0();
#pragma unroll
      for (int i = 0; i < 8; ++i) {
        const unsigned a = sg + q * 1024 + ((128 * i + 64 * hh + 32 * blk + 8 * pp) ^ (q << 6));
        const v4u f = trfrag(a, a + 4096);
        st_sc1(f, rs, hh * 1024 + wl * 512 + r * 16, WS_H + (i * 128 + jb) * 2048);
      }
      wait_vm<0>();
      if (lane < 8) __hip_atomic_store((gu64*)(p.ws + WS_HF + (2 * jb + wl) * 8 + lane * REPB), tag, __ATOMIC_RELAXED, __HIP_MEMORY_SCOPE_AGENT);
      TS(2);
    } else {
      {
        int pg = ((idx - 16) * 6 + w) * 64 + lane;
        const char* a;
        if (pg < 1376) a = p.w2 + (size_t)pg * 4096;
        else if (pg < 2752) a = p.w1 + (size_t)(pg - 1376) * 4096;
        else if (pg < 4416) a = p.ws + (size_t)(pg - 2752) * 4096 + 2048;
        else if (pg < 4928) a = p.out + (size_t)(pg - 4416) * 4096;
        else if (pg < 5056) a = p.wo + (size_t)(pg - 4928) * 4096;
        else if (pg < 5440) a = p.wqkv + (size_t)(pg - 5056) * 4096;
        else if (pg < 5952) a = p.x + (size_t)(pg - 5440) * 4096;
        else a = p.b1;
        asm volatile("global_load_dword %0, %1, off" : "=v"(touch) : "v"(a) : "memory");
      }
      if (gemm2) {
#pragma unroll 1
        for (int u = w; u < 48; u += 6) issue(u >> 4, (u >> 2) & 3, u & 3);
      }
      wait_vm<0>();
      PIN(touch); PIN(touch2);
      tag = 0;
    }
    if (gemm2) {
      const unsigned tb = (8 * hh + q) * 192 + 32 * blk + 8 * pp + wh * 12288;
      const unsigned ta[3] = {opaque(tb), opaque(tb + STG), opaque(tb + 2 * STG)};
      v4u wr[33][3], hr[32];
      auto rd = [&](int i, int fb) {
        const unsigned a = ta[(i >> 2) % 3] + (i & 3) * 3072 + 64 * fb;
        wr[i][fb] = trfrag(a, a + 768);
      };
      auto ldh = [&](int i) {
        hr[i] = ld_sc1(rs, lane * 16 + (i & 1) * 2048, WS_H + (dt * 128 + 32 * wh + 4 * (((i >> 2) + rot) & 7) + (i & 2)) * 2048 + wl * 1024);
      };
      const int nht = w < 4 ? 2 : 1, ht0 = w < 4 ? 2 * w : 4 + w;
      v2u bb[2][2];
#pragma unroll
      for (int e = 0; e < 2; ++e) {
        const char* bp = p.b1 + (f0 + 16 * ((ht0 + (e < nht ? e : 0)) >> 1) + 4 * hh) * 2;
        bb[e][0] = *(const v2u*)bp; bb[e][1] = *(const v2u*)(bp + 16);
      }
      if (w == 7) {
#pragma unroll 1
        for (unsigned spins = 0; spins < SPIN_CAP; ++spins) {
          const v4u a = ld_sc1(rs, lane * 16, WS_HF + rep), c = ld_sc1(rs, lane * 16 + 1024, WS_HF + rep);
          const unsigned tl = (unsigned)tag, th = (unsigned)(tag >> 32);
          const bool ok = a.x == tl && a.y == th && a.z == tl && a.w == th && c.x == tl && c.y == th && c.z == tl && c.w == th;
          if (__all(ok)) break;
          __builtin_amdgcn_s_sleep(2);
          asm volatile("" ::: "memory");
        }
      }
      wait_vm<0>();
      bar();
      TS(3);
#pragma unroll
      for (int i = 0; i < DH; ++i) ldh(i);
      rd(0, 0); rd(0, 1); rd(0, 2);
      SB();
      f32x16 acc[3] = {zero, zero, zero};
#pragma unroll
      for (int i = 0; i < 32; ++i) {
        const int s = i >> 2, k = i & 3;
        if (k == 3) {
          CYC_BEGIN(ta_);
          switch (s) {
#define BW(n) case n: wait_vm<b_wait(n)>(); break;
            BW(2) BW(3) BW(4) BW(5) BW(6)
#undef BW
          }
          CYC_END(ta_, dbg0);
          CYC_BEGIN(tc_);
          bar();
          CYC_END(tc_, dbg2);
        }
        SB();
        acc[0] = mfma(wr[i][0], hr[i], acc[0]);
        SB();
        if (i + 1 < 32) { rd(i + 1, 0); rd(i + 1, 1); rd(i + 1, 2); }
        SB();
        acc[1] = mfma(wr[i][1], hr[i], acc[1]);
        SB();
        if (b_glds(i)) { CYC_BEGIN(tb_); if (k == 3) issue(s + 3, wh, 2 * wl); else issue(s + 2, wh, 2 * wl + 1); CYC_END(tb_, dbg1); }
        if (i == 24) { PIN(hr[31]); }
        if (i >= 24) p3_issue(s3, i - 24, w);
        SB();
        acc[2] = mfma(wr[i][2], hr[i], acc[2]);
        SB();
        if (b_hld(i)) ldh(i + DH);
        SB();
      }
      TS(4);
#pragma unroll
      for (int fb = 0; fb < 3; ++fb)
#pragma unroll
        for (int qd = 0; qd < 4; ++qd)
          lds_w128(w * 12288 + (fb * 4 + qd) * 1024 + lane * 16, f4(acc[fb][4 * qd], acc[fb][4 * qd + 1], acc[fb][4 * qd + 2], acc[fb][4 * qd + 3]));
      bar();
#pragma unroll
      for (int e = 0; e < 2; ++e) {
        if (e < nht) {
          const int ht = ht0 + e;
          const unsigned a = (ht & 1) * 12288 + (ht >> 1) * 2048 + lane * 16;
          float s[8] = {bflo(bb[e][0].x), bfhi(bb[e][0].x), bflo(bb[e][0].y), bfhi(bb[e][0].y), bflo(bb[e][1].x), bfhi(bb[e][1].x), bflo(bb[e][1].y), bfhi(bb[e][1].y)};
#pragma unroll
          for (int ww = 0; ww < 4; ++ww) {
            const v4u lo = lds_r128(a + ww * 24576), hi = lds_r128(a + ww * 24576 + 1024);
#pragma unroll
            for (int j = 0; j < 4; ++j) { s[j] += __uint_as_float(lo[j]); s[4 + j] += __uint_as_float(hi[j]); }
          }
          const v4u uf = {pk(gelu(s[0]), gelu(s[1])), pk(gelu(s[2]), gelu(s[3])), pk(gelu(s[4]), gelu(s[5])), pk(gelu(s[6]), gelu(s[7]))};
          if (ft < 14 || ht >= 8) st_sc1(uf, rs, lane * 16, WS_U + ((dt * 86 + (f0 >> 4) + (ht >> 1)) * 2 + (ht & 1)) * 1024);
        }
      }
      publish(p.ws, WS_UF + (dt * 16 + ft) * 8, tag, tid);
      TS(6);
    } else {
      bar();
#pragma unroll 1
      for (int i = 0; i < 8; ++i) p3_issue(s3, i, w);
      wait_vm<0>();
      bar();
    }
    p3_have = 8;
  }

  {
#pragma unroll 1
    for (int i = p3_have; i < 20; ++i) p3_issue(s3, i, w);
    const int srow = 64 * st64 + (tid >> 3);
    const v4u xres = *(const v4u*)(p.x + (size_t)srow * 1024 + dt3 * 128 + (tid & 7) * 16);
    const float b2v = bflo(*(const unsigned short*)(p.b2 + srow * 2));
    const unsigned tb = wh * 2048 + (4 * hh + q) * 128 + ((32 * blk + 8 * pp) ^ ((q >> 1) << 6));
    const unsigned t0[2] = {opaque(s3.b0 + tb), opaque(s3.b0 + (tb ^ 64))};
    const unsigned t1[2] = {opaque(s3.b1 + tb), opaque(s3.b1 + (tb ^ 64))};
    const unsigned t2[2] = {opaque(s3.b2 + tb), opaque(s3.b2 + (tb ^ 64))};
    const unsigned tw = opaque(s3.b0 + (tb ^ (wl << 6)));
    v4u wof[8], wr[31][2], ar[8][2], ur[22];
    auto rd = [&](int i, int c) {
      const int j = i < 20 ? i : i - 20;
      const unsigned a = j < 8 ? t0[c] + j * ROW : j < 15 ? t1[c] + (j - 8) * ROW : t2[c] + (j - 15) * ROW;
      wr[i][c] = trfrag(a, a + 1024);
    };
#pragma unroll
    for (int i = 0; i < 8; ++i) wof[i] = trfrag(tw + i * ROW, tw + i * ROW + 1024);
    bar();
#pragma unroll
    for (int i = 20; i < 28; ++i) p3_issue(s3, i, w);
    TS(7);
    if (w == 7) poll(p.ws, WS_OF + rep, (st64 >> 1) * 8, (st64 >> 1) * 8 + 8, tag, lane);
    wait_vm<0>();
    bar();
    TS(8);
#pragma unroll
    for (int i = 0; i < 8; ++i) {
      const unsigned so = WS_O + (st64 * 32 + 8 * wh + i) * 2048;
      ar[i][0] = ld_sc1(rs, lane * 16, so); ar[i][1] = ld_sc1(rs, lane * 16 + 1024, so);
    }
    rd(8, 0); rd(8, 1); rd(9, 0); rd(9, 1);
    bar();
    p3_issue(s3, 28, w); p3_issue(s3, 29, w);
    SB();
    f32x16 acc[2] = {zero, zero};
#pragma unroll
    for (int i = 0; i < 8; ++i) {
      acc[0] = mfma(wof[i], ar[i][0], acc[0]);
      acc[1] = mfma(wof[i], ar[i][1], acc[1]);
      SB();
    }
    if (w == 7) poll(p.ws, WS_UF + rep, dt3 * 16, dt3 * 16 + 15, tag, lane);
    wait_vm<0>();
    bar();
    TS(9);
    auto ldu = [&](int k) {
      const int kk = k < s3.nu ? k : s3.nu - 1;
      ur[k] = ld_sc1(rs, lane * 16, WS_U + (dt3 * 86 + ks0 + kk) * 2048 + wl * 1024);
    };
#pragma unroll
    for (int k = 0; k < DU; ++k) ldu(k);
    SB();
#pragma unroll
    for (int k = 0; k < 22; ++k) {
      const int i = k + 8;
      if (k < 21 || wh < 2) {
        acc[0] = mfma(ur[k], wr[i][0], acc[0]);
        SB();
        if (i + 2 < 30) { rd(i + 2, 0); rd(i + 2, 1); }
        SB();
        acc[1] = mfma(ur[k], wr[i][1], acc[1]);
        SB();
        if (k + DU < 22) ldu(k + DU);
        SB();
      }
    }
    TS(10);
    bar();
#pragma unroll
    for (int c = 0; c < 2; ++c)
#pragma unroll
      for (int qd = 0; qd < 4; ++qd)
        lds_w128(wh * 17408 + (32 * c + r) * 272 + (32 * wl + 8 * qd + 4 * hh) * 4, f4(acc[c][4 * qd], acc[c][4 * qd + 1], acc[c][4 * qd + 2], acc[c][4 * qd + 3]));
    bar();
    {
      float s[8];
#pragma unroll
      for (int e = 0; e < 4; ++e) { s[2 * e] = bflo(xres[e]) + b2v; s[2 * e + 1] = bfhi(xres[e]) + b2v; }
#pragma unroll
      for (int ww = 0; ww < 4; ++ww) {
        const unsigned a = ww * 17408 + (tid >> 3) * 272 + (tid & 7) * 32;
        const v4u lo = lds_r128(a), hi = lds_r128(a + 16);
#pragma unroll
        for (int j = 0; j < 4; ++j) { s[j] += __uint_as_float(lo[j]); s[4 + j] += __uint_as_float(hi[j]); }
      }
      *(v4u*)(p.out + (size_t)srow * 1024 + dt3 * 128 + (tid & 7) * 16) = v4u{pk(s[0], s[1]), pk(s[2], s[3]), pk(s[4], s[5]), pk(s[6], s[7])};
    }
    if (b == 0 && tid == 448) *(u64*)p.ws = tag;
  }
}
}

extern "C" void kernel_launch(void* const* d_in, const int* in_sizes, int n_in, void* d_out, int out_size, void* d_ws,
                              size_t ws_size, hipStream_t stream) {
  hk::Params p;
  p.x = (const char*)d_in[0]; p.wqkv = (const char*)d_in[1]; p.wphi = (const char*)d_in[2]; p.wo = (const char*)d_in[3];
  p.w1 = (const char*)d_in[5]; p.b1 = (const char*)d_in[6]; p.w2 = (const char*)d_in[7]; p.b2 = (const char*)d_in[8];
  p.out = (char*)d_out; p.ws = (char*)d_ws;
  (void)hipFuncSetAttribute((const void*)hk::fused, hipFuncAttributeMaxDynamicSharedMemorySize, hk::LDS_BYTES);
  hk::fused<<<256, 512, hk::LDS_BYTES, stream>>>(p);
}
```

```cpp
#include <hip/hip_runtime.h>
#include <stdint.h>

#define GAS __attribute__((address_space(1)))
#define LAS __attribute__((address_space(3)))
#define DI __device__ __forceinline__
typedef short bf16x8 __attribute__((ext_vector_type(8)));
typedef short s16x4 __attribute__((ext_vector_type(4)));
typedef __bf16 bf2 __attribute__((ext_vector_type(2)));
typedef float f32x16 __attribute__((ext_vector_type(16)));
typedef float f32x2 __attribute__((ext_vector_type(2)));
typedef unsigned v4u __attribute__((ext_vector_type(4)));
typedef unsigned v2u __attribute__((ext_vector_type(2)));
typedef unsigned long long u64;
typedef GAS u64 gu64;

namespace hk {
constexpr unsigned REPB = 8192;
constexpr unsigned WS_HF = 4096, WS_CF = WS_HF + 1024, WS_KF = WS_CF + 1024, WS_OF = WS_KF + 1024, WS_UF = WS_OF + 1024;
constexpr unsigned WS_CS = 73728, WS_ST = 98304, WS_ZP = 131072, WS_KVP = 196608, WS_TS = 768 * 1024;
constexpr unsigned WS_H = 1u << 20, WS_U = 3u << 20, WS_O = 9u << 19, WS_BYTES = 13u << 19;
constexpr unsigned REG = 40960, ROW = 8192, STG = 49152, LDS_BYTES = 163840;
constexpr unsigned MISC = 19 * ROW;
constexpr unsigned W_CB = MISC, W_PC = MISC + 4, W_GO = MISC + 8, W_FX = MISC + 12, W_LN = MISC + 16, W_CS = MISC + 20,
                   W_LF = MISC + 32  , W_FR = MISC + 48  , W_TAG = MISC + 64, X_Z = MISC + 256, X_CS = MISC + 2560;
constexpr unsigned SPIN_CAP = 1u << 14, LSPIN_CAP = 1u << 20;
constexpr int AUX_SC1 = 16;
constexpr float LOG2E = 1.4426950408889634f;

struct Params {
  const char *x, *wqkv, *wphi, *wo, *w1, *b1, *w2, *b2;
  char *out, *ws;
};

DI f32x16 mfma(v4u a, v4u b, f32x16 c) {
  return __builtin_amdgcn_mfma_f32_32x32x16_bf16(__builtin_bit_cast(bf16x8, a), __builtin_bit_cast(bf16x8, b), c, 0, 0, 0);
}
DI unsigned pk(float a, float b) { bf2 v = {(__bf16)a, (__bf16)b}; return __builtin_bit_cast(unsigned, v); }
DI unsigned pk2(f32x2 a) { return pk(a.x, a.y); }
DI float bflo(unsigned u) { return __uint_as_float(u << 16); }
DI float bfhi(unsigned u) { return __uint_as_float(u & 0xffff0000u); }
DI float uf(unsigned u) { return __uint_as_float(u); }
DI unsigned fu(float f) { return __float_as_uint(f); }
template <int O> DI v4u pack8(const f32x16& x) {
  return v4u{pk(x[O], x[O + 1]), pk(x[O + 2], x[O + 3]), pk(x[O + 4], x[O + 5]), pk(x[O + 6], x[O + 7])};
}
DI v4u f4(float a, float b, float c, float d) { return v4u{fu(a), fu(b), fu(c), fu(d)}; }
DI v2u trr(unsigned a) { return __builtin_bit_cast(v2u, __builtin_amdgcn_ds_read_tr16_b64_v4i16((LAS s16x4*)(size_t)a)); }
DI v4u trfrag(unsigned a0, unsigned a1) { v2u lo = trr(a0), hi = trr(a1); return v4u{lo.x, lo.y, hi.x, hi.y}; }
DI v4u lds_r128(unsigned a) { return *(LAS v4u*)(size_t)a; }
DI v2u lds_r64(unsigned a) { return *(LAS v2u*)(size_t)a; }
DI float lds_r32(unsigned a) { return *(LAS float*)(size_t)a; }
DI void lds_w128(unsigned a, v4u v) { *(LAS v4u*)(size_t)a = v; }
DI void lds_w64(unsigned a, v2u v) { *(LAS v2u*)(size_t)a = v; }
DI void lds_w32(unsigned a, float v) { *(LAS float*)(size_t)a = v; }
DI void glds2(unsigned lds, const char* sb, unsigned v0, unsigned v1) {
  asm volatile("s_mov_b32 m0, %0\n\ts_nop 0\n\tglobal_load_lds_dwordx4 %2, %1\n\tglobal_load_lds_dwordx4 %3, %1 offset:1024"
               ::"s"(lds), "s"(sb), "v"(v0), "v"(v1) : "memory");
}
DI void glds3(unsigned lds, const char* sb, unsigned v0, unsigned v1, unsigned v2) {
  asm volatile("s_mov_b32 m0, %0\n\ts_nop 0\n\tglobal_load_lds_dwordx4 %2, %1\n\tglobal_load_lds_dwordx4 %3, %1 offset:1024\n\t"
               "global_load_lds_dwordx4 %4, %1 offset:2048"
               ::"s"(lds), "s"(sb), "v"(v0), "v"(v1), "v"(v2) : "memory");
}
DI void glds4(unsigned lds, const char* sb, unsigned v0, unsigned v1, unsigned v2, unsigned v3) {
  asm volatile("s_mov_b32 m0, %0\n\ts_nop 0\n\tglobal_load_lds_dwordx4 %2, %1\n\tglobal_load_lds_dwordx4 %3, %1 offset:1024\n\t"
               "global_load_lds_dwordx4 %4, %1 offset:2048\n\tglobal_load_lds_dwordx4 %5, %1 offset:3072"
               ::"s"(lds), "s"(sb), "v"(v0), "v"(v1), "v"(v2), "v"(v3) : "memory");
}
#define SB() __builtin_amdgcn_sched_barrier(0)
#define PIN(x) asm volatile("" : "+v"(x))
#define MEMFENCE() asm volatile("" ::: "memory")
DI unsigned opaque(unsigned v) { asm volatile("" : "+v"(v)); return v & 0x3ffffu; }
DI unsigned rfl(unsigned v) { return __builtin_amdgcn_readfirstlane(v); }
template <int N> DI void wait_vm() { asm volatile("s_waitcnt vmcnt(%0)" ::"n"(N) : "memory"); }
DI void wait_lgkm0() { asm volatile("s_waitcnt lgkmcnt(0)" ::: "memory"); }
DI void bar() { asm volatile("s_waitcnt lgkmcnt(0)" ::: "memory"); __builtin_amdgcn_s_barrier(); MEMFENCE(); }
DI v4u ld_sc1(__amdgpu_buffer_rsrc_t rs, unsigned voff, unsigned soff) { return __builtin_amdgcn_raw_buffer_load_b128(rs, voff, soff, AUX_SC1); }
DI void st_sc1(v4u v, __amdgpu_buffer_rsrc_t rs, unsigned voff, unsigned soff) { __builtin_amdgcn_raw_buffer_store_b128(v, rs, voff, soff, AUX_SC1); }
DI float addf(float a, float b) { float r; asm("v_add_f32 %0, %1, %2" : "=v"(r) : "v"(a), "v"(b)); return r; }

DI void lds_inc(unsigned word) {
  asm volatile("s_waitcnt lgkmcnt(0)\n\ts_mov_b64 exec, 1\n\tds_add_u32 %0, %1\n\ts_mov_b64 exec, -1" ::"v"(word), "v"(1u) : "memory");
}
DI unsigned lds_inc_rtn(unsigned word) {
  unsigned old;
  asm volatile("s_waitcnt lgkmcnt(0)\n\ts_mov_b64 exec, 1\n\tds_add_rtn_u32 %0, %1, %2\n\ts_mov_b64 exec, -1\n\ts_waitcnt lgkmcnt(0)"
               : "=&v"(old) : "v"(word), "v"(1u) : "memory");
  return rfl(old);
}
template <int SLEEP = 0> DI void lds_wait(unsigned word, unsigned want) {
#pragma unroll 1
  for (unsigned spins = 0; spins < LSPIN_CAP; ++spins) {
    if (rfl(__hip_atomic_load((LAS unsigned*)(size_t)word, __ATOMIC_RELAXED, __HIP_MEMORY_SCOPE_WORKGROUP)) >= want) break;
    if (SLEEP) __builtin_amdgcn_s_sleep(SLEEP);
  }
  MEMFENCE();
}
DI void cbar(unsigned n) { lds_inc(W_CB); lds_wait(W_CB, 8 * n); }
DI void flag_store(char* ws, unsigned off, u64 tag, int lane) {
  if (lane < 8) __hip_atomic_store((gu64*)(ws + off + lane * REPB), tag, __ATOMIC_RELAXED, __HIP_MEMORY_SCOPE_AGENT);
  MEMFENCE();
}
DI void publish(char* ws, unsigned off, u64 tag, int lane) {
  wait_vm<0>();
  if ((lds_inc_rtn(W_PC) & 7) == 7) flag_store(ws, off, tag, lane);
}
DI void poll(char* ws, unsigned off, u64 tag) {
  gu64* p = (gu64*)(ws + off);
#pragma unroll 1
  for (unsigned spins = 0; spins < SPIN_CAP; ++spins) {
    u64 v = __hip_atomic_load(p, __ATOMIC_RELAXED, __HIP_MEMORY_SCOPE_AGENT);
    if (__all(v == tag)) break;
    __builtin_amdgcn_s_sleep(1);
  }
  __builtin_amdgcn_fence(__ATOMIC_ACQUIRE, "wavefront");
  MEMFENCE();
}
DI void gate(char* ws, unsigned off, u64 tag, int w, unsigned n) {
  if (w == 7) { poll(ws, off, tag); lds_inc(W_GO); }
  else lds_wait<1>(W_GO, n);
}

template <int CTRL> DI float dpp_add(float v) { return v + uf(__builtin_amdgcn_update_dpp(0u, fu(v), CTRL, 0xf, 0xf, true)); }
DI float row_sum(float v) { return dpp_add<0x140>(dpp_add<0x141>(dpp_add<0x4E>(dpp_add<0xB1>(v)))); }
DI float sw16_add(float a, float b) { auto r = __builtin_amdgcn_permlane16_swap(fu(a), fu(b), false, false); return uf(r[0]) + uf(r[1]); }
DI float sw32_add(float a, float b) { auto r = __builtin_amdgcn_permlane32_swap(fu(a), fu(b), false, false); return uf(r[0]) + uf(r[1]); }
DI void sw32_split(float m, float& a, float& b) { auto q = __builtin_amdgcn_permlane32_swap(fu(m), fu(m), false, false); a = uf(q[0]); b = uf(q[1]); }
DI float mx(float a, float b) { return __builtin_elementwise_maximum(a, b); }
DI float mn(float a, float b) { return __builtin_elementwise_minimum(a, b); }
template <int O> DI void hh_exp(const f32x16& P, float c, f32x2 (&ep)[4], f32x2 (&en)[4]) {
  const float hi = mx(mx(mx(P[O], P[O + 1]), P[O + 2]), mx(mx(mx(P[O + 3], P[O + 4]), P[O + 5]), mx(P[O + 6], P[O + 7])));
  const float lo = mn(mn(mn(P[O], P[O + 1]), P[O + 2]), mn(mn(mn(P[O + 3], P[O + 4]), P[O + 5]), mn(P[O + 6], P[O + 7])));
  auto r = __builtin_amdgcn_permlane32_swap(fu(hi), fu(-lo), false, false);
  float bp, bn;
  sw32_split(mx(uf(r[0]), uf(r[1])) * -c, bp, bn);
  const f32x2 c2 = {c, c}, bp2 = {bp, bp}, bn2 = {bn, bn};
#pragma unroll
  for (int j = 0; j < 4; ++j) {
    const f32x2 pv = {P[O + 2 * j], P[O + 2 * j + 1]};
    const f32x2 a = __builtin_elementwise_fma(pv, c2, bp2), b = __builtin_elementwise_fma(pv, -c2, bn2);
    ep[j] = f32x2{__builtin_amdgcn_exp2f(a.x), __builtin_amdgcn_exp2f(a.y)};
    en[j] = f32x2{__builtin_amdgcn_exp2f(b.x), __builtin_amdgcn_exp2f(b.y)};
  }
}
template <int O> DI void hedgehog(const f32x16& P, float c, v4u& fp, v4u& fn) {
  f32x2 ep[4], en[4];
  hh_exp<O>(P, c, ep, en);
  const f32x2 s2 = (ep[0] + ep[1]) + (ep[2] + ep[3]), t2 = (en[0] + en[1]) + (en[2] + en[3]);
  float rp, rn;
  sw32_split(__builtin_amdgcn_rcpf(sw32_add(s2.x + s2.y, t2.x + t2.y)), rp, rn);
  const f32x2 rp2 = {rp, rp}, rn2 = {rn, rn};
  fp = v4u{pk2(ep[0] * rp2), pk2(ep[1] * rp2), pk2(ep[2] * rp2), pk2(ep[3] * rp2)};
  fn = v4u{pk2(en[0] * rn2), pk2(en[1] * rn2), pk2(en[2] * rn2), pk2(en[3] * rn2)};
}
DI void split3(float v, unsigned& a, unsigned& b, unsigned& c) {
  a = pk(v, 0.f); float r = v - bflo(a);
  b = pk(r, 0.f); r -= bflo(b);
  c = pk(r, 0.f);
}
DI float gelu(float x) {
  const float e = x * __builtin_fmaf(x * x, -2.f * LOG2E * 0.044715f * 0.7978845608028654f, -2.f * LOG2E * 0.7978845608028654f);
  return x * __builtin_amdgcn_rcpf(1.f + __builtin_amdgcn_exp2f(e));
}
DI u64 ld_epoch(const char* ws) {
  u64 e;
  asm volatile("s_load_dwordx2 %0, %1, 0x0\n\ts_waitcnt lgkmcnt(0)" : "=s"(e) : "s"(ws) : "memory");
  return e;
}
DI u64 mix64(u64 z) {
  z += 0x9E3779B97F4A7C15ull; z = (z ^ (z >> 30)) * 0xBF58476D1CE4E5B9ull; z = (z ^ (z >> 27)) * 0x94D049BB133111EBull;
  return z ^ (z >> 31);
}

#define BT(k)
#define CB(v)
#define CE(v, a)
#define TS(k) do { } while (0)

constexpr int DH = 12;
constexpr int DU = 12;

__global__ __launch_bounds__(768) void fused(Params p) {
  extern __shared__ __attribute__((aligned(16))) char smem[];
  const int tid = threadIdx.x, lane = tid & 63, w = rfl(tid >> 6);
  const int b = blockIdx.x, xg = b & 7, idx = b >> 3, sx = xg >> 1, gx = xg & 1;
  const bool roleA = idx < 16;
  asm volatile("" ::"v"((LAS char*)smem));
  const int i16 = idx & 15;
  const int st64 = 8 * sx + (i16 & 7);
  const int dt3 = 4 * gx + (((i16 >> 3) + 2 * (i16 & 1) + 2 * (idx >> 4)) & 3);
  const unsigned ga = roleA ? 8 * ROW : 12 * ROW, gc = roleA ? 15 * ROW : 8 * ROW;
  const unsigned rep = xg * REPB;
  const int st = 4 * sx + (idx & 3), g = 4 * gx + ((idx >> 2) & 3);
  const int jb = 32 * sx + 16 * gx + i16;
  const int ftl = i16 >> 2, dt = 4 * gx + (i16 & 3);
  const int skip = ftl < 3 ? 0 : sx < 3 ? 1 : 2;
  const int f0 = 32 * (11 * sx + 3 * ftl - skip);
  const int rotb = 0;

  if (w >= 8) {
    const int li = w - 8;
    const unsigned r8 = lane >> 3, ch4 = ((lane & 7) ^ (((lane >> 4) & 1) << 2)) * 16;
    const unsigned vo0 = r8 * 1024 + ch4, vo1 = vo0 + 8 * 1024 - 1024, v20 = r8 * 4096 + ch4, v21 = v20 + 8 * 4096 - 1024;
    auto w2_issue = [&](int lo, int hi) {
#pragma unroll 1
      for (int i = lo; i < hi; ++i) {
        const unsigned row = rfl((i < 7 ? ga + i * ROW : i < 10 ? ga + (i - 7) * ROW : i < 18 ? (i - 10) * ROW : gc + (i - 18) * ROW) + li * 2048);
        const int k = 22 * li + i;
        glds2(row, p.w2 + (size_t)(k < 86 ? k : 85) * 65536 + st64 * 128, v20, v21);
      }
    };
    auto wo_issue = [&]() {
#pragma unroll 1
      for (int i = 0; i < 8; ++i) {
        const unsigned a = rfl(roleA ? i * ROW + li * 2048 : (i < 6 ? 2 * STG + i * 2048 : STG - 4096 + i * 2048) + li * 12288);
        glds2(a, p.wo + (size_t)(128 * li + 16 * i) * 1024 + dt3 * 128, vo0, vo1);
      }
    };
    const unsigned LFw = W_LF + 4 * li, FRw = W_FR + 4 * li;
    if (roleA) {
      const int rot = 0;
      const unsigned xc = ((lane & 7) ^ (lane >> 4)) * 16;
      const unsigned vx0 = r8 * 1024 + xc, vx1 = r8 * 1024 + (xc ^ 64) + 8192 - 1024, vx2 = vx0 + 16384 - 2048, vx3 = vx1 + 16384 - 2048;
      const unsigned vw0 = r8 * 3072 + ch4, vw1 = vw0 + 24576 - 1024;
      const char* xs = p.x + (size_t)(128 * st + 32 * li) * 1024;
      const char* wsrc = p.wqkv + (size_t)(16 * li) * 3072 + g * 128;
      unsigned sl = 0;
#pragma unroll 1
      for (int c = 0; c < 8; ++c) {
        const int kc = (c + rot) & 7;
        CB(ti_);
        glds4(sl + li * 4096, xs + kc * 128, vx0, vx1, vx2, vx3);
        const char* wb = wsrc + (size_t)kc * 64 * 3072;
        glds2(sl + 16384 + li * 2048, wb, vw0, vw1);
        glds2(sl + 24576 + li * 2048, wb + 1024, vw0, vw1);
        glds2(sl + 32768 + li * 2048, wb + 2048, vw0, vw1);
        sl = sl == 2 * REG ? 0 : sl + REG;
        if (c >= 2) { CE(ti_, l0); }
        if (c >= 1) { CB(tw_); wait_vm<10>(); if (c >= 2) { CE(tw_, l1); } CB(tb_); bar(); if (c >= 2) { CE(tb_, l2); } }
      }
      if (li == 0)
        poll(p.ws, rep + (lane < 8 ? WS_HF + (8 * st + lane) * 8 : WS_CF + (4 * g + (lane < 11 ? lane - 8 : 0)) * 8), mix64(ld_epoch(p.ws)));
      wait_vm<0>(); bar();
      bar();
      wo_issue(); w2_issue(0, 7);
      wait_vm<8>(); lds_inc(LFw);
      lds_wait<1>(FRw, 2); w2_issue(7, 18);
      wait_vm<16>(); lds_inc(LFw);
      lds_wait<1>(W_FX, 8); w2_issue(18, 22);
    } else {
      unsigned vw[3];
#pragma unroll
      for (int n = 0; n < 3; ++n) { const unsigned id = 64 * n + lane, row = id / 12, ch = id % 12; vw[n] = row * 2752 + ch * 16 - 1024 * n; }
      const char* w1s = p.w1 + f0 * 2 + (size_t)(512 * li) * 2752;
      unsigned sl = li * 12288;
#pragma unroll 1
      for (int s = 0; s < 8; ++s) {
        const char* sb = w1s + (size_t)(64 * ((s + rotb) & 7)) * 2752;
        CB(ti_);
#pragma unroll
        for (int k = 0; k < 4; ++k) glds3(sl + k * 3072, sb + k * 16 * 2752, vw[0], vw[1], vw[2]);
        sl = sl >= 2 * STG ? sl - 2 * STG : sl + STG;
        if (s >= 3) { CE(ti_, l0); }
        if (s == 0) {
          bar();
          wo_issue();
          wait_vm<0>(); lds_inc(LFw);
          lds_wait<1>(FRw, 2);
        }
        if (s == 2) bar();
        if (s >= 2) { CB(tw_); wait_vm<12>(); if (s >= 3) { CE(tw_, l1); } CB(tb_); bar(); if (s >= 3) { CE(tb_, l2); } }
      }
      w2_issue(0, 7);
      wait_vm<14>(); bar();
      wait_vm<8>(); bar();
      lds_wait<1>(FRw, 4); w2_issue(7, 10);
      lds_wait<1>(W_FX, 8); w2_issue(10, 22);
      wait_vm<24>(); lds_inc(LFw);
    }
    wait_vm<0>(); lds_inc(LFw);
    return;
  }

  const int r = lane & 31, hh = lane >> 5, blk = (lane >> 4) & 1, q = (lane >> 2) & 3, pp = lane & 3;
  const int wh = w >> 1, wl = w & 1;
  TS(0);
  u64 tag;
  const __amdgpu_buffer_rsrc_t rs = __builtin_amdgcn_make_buffer_rsrc(p.ws, 0, WS_BYTES, 0x00020000);
  f32x16 zero;
#pragma unroll
  for (int e = 0; e < 16; ++e) zero[e] = 0.f;
  if (w == 0 && lane < 16) lds_w32(MISC + lane * 4, 0.f);
  const unsigned tl3 = (4 * hh + q) * 128 + ((32 * blk + 8 * pp) ^ ((q >> 1) << 6)), tb3 = wh * 2048 + tl3;
  const unsigned LFw = W_LF + 4 * wh, FRw = W_FR + 4 * wh;
  v4u wof[8], wr3[22][2];
  auto rd_w2a = [&]() {
#pragma unroll
    for (int i = 0; i < 3; ++i)
#pragma unroll
      for (int c = 0; c < 2; ++c) { const unsigned a = ga + i * ROW + (tb3 ^ (c << 6)); wr3[i][c] = trfrag(a, a + 1024); }
  };
  unsigned ngo;

  if (roleA) {
    const int rg = wh, cg = wl, pair = 2 * g + cg;
    v4u aphi0, aphi1, id0, id1;
    {
      const unsigned short* wp = (const unsigned short*)p.wphi + (2 * pair + (r >> 4)) * 256 + (r & 15) + 64 * hh;
      unsigned e[8];
#pragma unroll
      for (int j = 0; j < 8; ++j) e[j] = wp[(8 * (j >> 2) + (j & 3)) * 16];
      tag = mix64(ld_epoch(p.ws));
      const v4u z4 = {0, 0, 0, 0};
      const v4u fphi = {e[0] | (e[1] << 16), e[2] | (e[3] << 16), e[4] | (e[5] << 16), e[6] | (e[7] << 16)};
      aphi0 = r < 16 ? fphi : z4; aphi1 = r < 16 ? z4 : fphi;
      const int rr = (r & 15) - 4 * hh;
      v4u e1;
      e1.x = rr == 0 ? 0x3f80u : rr == 1 ? 0x3f800000u : 0u; e1.y = rr == 2 ? 0x3f80u : rr == 3 ? 0x3f800000u : 0u;
      e1.z = rr == 8 ? 0x3f80u : rr == 9 ? 0x3f800000u : 0u; e1.w = rr == 10 ? 0x3f80u : rr == 11 ? 0x3f800000u : 0u;
      id0 = r < 16 ? e1 : z4; id1 = r < 16 ? z4 : e1;
      PIN(aphi0); PIN(aphi1); PIN(id0); PIN(id1);
    }
    f32x16 acc[3] = {zero, zero, zero};
    unsigned xa[4], wa[4];
#pragma unroll
    for (int k = 0; k < 4; ++k) {
      xa[k] = (32 * rg + r) * 128 + (unsigned)(((2 * k + hh) ^ ((r >> 1) & 7)) << 4);
      wa[k] = 16384 + (16 * k + 8 * hh + q) * 128 + ((64 * cg + 32 * blk + 8 * pp) ^ ((q >> 1) << 6));
    }
    v4u fx[2], fw[2][3], stv = {0, 0, 0, 0}, cf[3] = {stv, stv, stv};
    auto rdw = [&](unsigned a, int bf, int t) { fw[bf][t] = trfrag(a + t * 8192, a + t * 8192 + 512); };
    PIN(acc[0]); PIN(acc[1]); PIN(acc[2]);
    bar();
    TS(1);
    fx[0] = lds_r128(xa[0]); rdw(wa[0], 0, 0); rdw(wa[0], 0, 1); rdw(wa[0], 0, 2);
    unsigned sl = 0;
#pragma unroll 1
    for (int c = 0; c < 8; ++c) {
#pragma unroll
      for (int k = 0; k < 4; ++k) {
        const int cb = k & 1, nb = cb ^ 1, nk = (k + 1) & 3;
        if (k == 3) {
          { CB(t_); bar(); CE(t_, d0); }
          if (c == 6) {
            stv = ld_sc1(rs, r * 16, WS_ST + (128 * st + 32 * rg) * 16);
#pragma unroll
            for (int t = 0; t < 3; ++t) cf[t] = ld_sc1(rs, r * 16 + (hh << 30), WS_CS + (512 * t + 32 * pair) * 16);
          }
          sl = sl == 2 * REG ? 0 : sl + REG;
        }
        SB();
        acc[0] = mfma(fw[cb][0], fx[cb], acc[0]);
        SB();
        fx[nb] = lds_r128(sl + xa[nk]);
        rdw(sl + wa[nk], nb, 0);
        SB();
        acc[1] = mfma(fw[cb][1], fx[cb], acc[1]);
        SB();
        rdw(sl + wa[nk], nb, 1);
        rdw(sl + wa[nk], nb, 2);
        SB();
        acc[2] = mfma(fw[cb][2], fx[cb], acc[2]);
        SB();
      }
    }
    TS(2);
    const unsigned EX = 15 * ROW;
    const float rstd = uf(stv.w);
    {
      const v4u mf = {stv.x, stv.y, stv.z, 0u};
      acc[1] = mfma(cf[1], mf, acc[1]); acc[2] = mfma(cf[2], mf, acc[2]); acc[0] = mfma(cf[0], mf, acc[0]);
    }
    const float cexp = rstd * LOG2E;
    {
      f32x16 P = mfma(aphi0, pack8<0>(acc[1]), zero);
      P = mfma(aphi1, pack8<8>(acc[1]), P);
      f32x16 vs;
#pragma unroll
      for (int e = 0; e < 16; ++e) vs[e] = acc[2][e] * rstd;
      f32x16 zv = mfma(pack8<0>(vs), id0, zero);
      zv = mfma(pack8<8>(vs), id1, zv);
      v4u fp, fn, gp, gn;
      hedgehog<0>(P, cexp, fp, fn);
      hedgehog<8>(P, cexp, gp, gn);
      f32x16 zf = mfma(fp, id0, zero), zg = mfma(gp, id0, zero);
      zf = mfma(fn, id1, zf); zg = mfma(gn, id1, zg);
      const v4u vt0 = pack8<0>(zv), vt1 = pack8<8>(zv);
      f32x16 kv0 = mfma(pack8<0>(zf), vt0, zero), kv1 = mfma(pack8<0>(zg), vt0, zero);
      kv0 = mfma(pack8<8>(zf), vt1, kv0); kv1 = mfma(pack8<8>(zg), vt1, kv1);
      f32x2 z0 = {zf[0], zf[1]}, z1 = {zg[0], zg[1]};
#pragma unroll
      for (int e = 2; e < 16; e += 2) { z0 += f32x2{zf[e], zf[e + 1]}; z1 += f32x2{zg[e], zg[e + 1]}; }
      lds_w32(X_Z + w * 256 + lane * 4, sw32_add(z0.x + z0.y, z1.x + z1.y));
#pragma unroll
      for (int e = 0; e < 16; ++e) kv0[e] = r < 16 ? kv0[e] : kv1[e];
#pragma unroll
      for (int i = 0; i < 4; ++i) lds_w128(EX + w * 4096 + i * 1024 + lane * 16, f4(kv0[4 * i], kv0[4 * i + 1], kv0[4 * i + 2], kv0[4 * i + 3]));
      cbar(1);
      if (rg < 2) {
        float s[8];
#pragma unroll
        for (int i = 0; i < 4; ++i) {
          const unsigned a = EX + (2 * i + cg) * 4096 + rg * 2048 + lane * 16;
          const v4u lo = lds_r128(a), hi = lds_r128(a + 1024);
#pragma unroll
          for (int e = 0; e < 4; ++e) { s[e] = i ? s[e] + uf(lo[e]) : uf(lo[e]); s[4 + e] = i ? s[4 + e] + uf(hi[e]) : uf(hi[e]); }
        }
        st_sc1(v4u{pk(s[0], s[1]), pk(s[2], s[3]), pk(s[4], s[5]), pk(s[6], s[7])}, rs, lane * 16, WS_KVP + ((pair * 16 + st) * 2 + rg) * 1024);
      } else if (rg == 2) {
        const unsigned a = X_Z + cg * 256 + lane * 4;
        const float zt = (lds_r32(a) + lds_r32(a + 512)) + (lds_r32(a + 1024) + lds_r32(a + 1536));
        __builtin_amdgcn_raw_buffer_store_b32(fu(zt), rs, lane * 4, WS_ZP + (pair * 16 + st) * 256, AUX_SC1);
      }
    }
    publish(p.ws, WS_KF + (g * 16 + st) * 8, tag, lane);
    TS(3);
    v4u fq[2][2];
    {
      f32x16 P = mfma(aphi0, pack8<0>(acc[0]), zero);
      P = mfma(aphi1, pack8<8>(acc[0]), P);
      hedgehog<0>(P, cexp, fq[0][0], fq[0][1]);
      hedgehog<8>(P, cexp, fq[1][0], fq[1][1]);
      PIN(fq[0][0]); PIN(fq[0][1]); PIN(fq[1][0]); PIN(fq[1][1]);
    }
    lds_wait(LFw, 1);
#pragma unroll
    for (int i = 0; i < 8; ++i) { const unsigned a = i * ROW + (tb3 ^ (wl << 6)); wof[i] = trfrag(a, a + 1024); }
    rd_w2a();
    lds_inc(FRw);
    TS(4);
    gate(p.ws, WS_KF + rep + (g * 16 + (lane & 15)) * 8, tag, w, 1);
    TS(5);
    {
      v4u kp[8];
#pragma unroll
      for (int i = 0; i < 8; ++i) kp[i] = ld_sc1(rs, lane * 16, WS_KVP + (pair * 32 + (rg & 1) + 2 * (8 * (rg >> 1) + i)) * 1024);
      if (rg == 0) {
        v4u zp[4];
#pragma unroll
        for (int i = 0; i < 4; ++i) zp[i] = ld_sc1(rs, lane * 16, WS_ZP + pair * 4096 + i * 1024);
        float zs[4];
#pragma unroll
        for (int e = 0; e < 4; ++e) {
          float t = (uf(zp[0][e]) + uf(zp[1][e])) + (uf(zp[2][e]) + uf(zp[3][e]));
          t = sw16_add(t, t); zs[e] = sw32_add(t, t);
        }
        lds_w64(X_Z + w * 256 + (lane & 15) * 8, v2u{pk(zs[0], zs[1]), pk(zs[2], zs[3])});
      }
      f32x16 Ssum = zero;
#pragma unroll
      for (int i = 0; i < 8; ++i) Ssum = mfma(id0, kp[i], Ssum);
      lds_w128(EX + w * 4096 + lane * 16, f4(Ssum[0], Ssum[1], Ssum[2], Ssum[3]));
      lds_w128(EX + w * 4096 + 1024 + lane * 16, f4(Ssum[4], Ssum[5], Ssum[6], Ssum[7]));
      cbar(2);
      v4u ah[2][2];
#pragma unroll
      for (int t = 0; t < 2; ++t) {
        const unsigned a = EX + (2 * t + cg) * 4096 + lane * 16;
        const v4u a0 = lds_r128(a), a1 = lds_r128(a + 1024), b0 = lds_r128(a + 16384), b1 = lds_r128(a + 16384 + 1024);
        float s[8];
#pragma unroll
        for (int e = 0; e < 4; ++e) { s[e] = uf(a0[e]) + uf(b0[e]); s[4 + e] = uf(a1[e]) + uf(b1[e]); }
        const v4u m = {pk(s[0], s[1]), pk(s[2], s[3]), pk(s[4], s[5]), pk(s[6], s[7])};
#pragma unroll
        for (int h = 0; h < 2; ++h) {
          const unsigned za = X_Z + cg * 256 + (32 * h + 16 * t + 4 * hh) * 2;
          const v2u lo = lds_r64(za), hi = lds_r64(za + 16);
          const v4u zf = {lo.x, lo.y, hi.x, hi.y};
          ah[h][t] = ((r < 16) == (h == 0)) ? m : zf;
        }
      }
      lds_inc(W_FX);
      const unsigned ob = WS_O + (((2 * st + (rg >> 1)) * 32 + 2 * pair) * 2 + (rg & 1)) * 1024;
      f32x16 N0 = mfma(ah[0][0], fq[0][0], zero), N1 = mfma(ah[1][0], fq[1][0], zero);
      N0 = mfma(ah[0][1], fq[0][1], N0); N1 = mfma(ah[1][1], fq[1][1], N1);
      const float r0 = __builtin_amdgcn_rcpf(N0[8] + 1e-6f), r1 = __builtin_amdgcn_rcpf(N1[0] + 1e-6f);
#pragma unroll
      for (int e = 0; e < 8; ++e) { N0[e] *= r0; N1[8 + e] *= r1; }
      st_sc1(pack8<0>(N0), rs, lane * 16, ob);
      st_sc1(pack8<8>(N1), rs, lane * 16, ob + 2048);
    }
    publish(p.ws, WS_OF + (st * 8 + g) * 8, tag, lane);
    TS(6);
    ngo = 1;
  } else {
    if (w >= 6) {
      const unsigned sg = 18 * ROW + wl * 4096;
      const char* xs = p.x + (size_t)(16 * jb + 8 * wl) * 1024;
      v4u xr[8];
#pragma unroll
      for (int i = 0; i < 8; ++i) xr[i] = *(const v4u*)(xs + i * 1024 + lane * 16);
      __builtin_amdgcn_s_setprio(3);
      bar();
      tag = mix64(ld_epoch(p.ws));
      if (w == 7 && lane == 0) lds_w64(W_TAG, v2u{(unsigned)tag, (unsigned)(tag >> 32)});
      f32x2 v[8][4];
      float a8[8];
#pragma unroll
      for (int i = 0; i < 8; ++i) {
#pragma unroll
        for (int e = 0; e < 4; ++e) v[i][e] = f32x2{bflo(xr[i][e]), bfhi(xr[i][e])};
        const f32x2 s = (v[i][0] + v[i][1]) + (v[i][2] + v[i][3]);
        const f32x2 sq = __builtin_elementwise_fma(v[i][0], v[i][0], v[i][1] * v[i][1]) + __builtin_elementwise_fma(v[i][2], v[i][2], v[i][3] * v[i][3]);
        a8[i] = sw32_add(s.x + s.y, sq.x + sq.y);
      }
      TS(1);
      float a4[4];
#pragma unroll
      for (int i = 0; i < 4; ++i) a4[i] = row_sum(sw16_add(a8[i], a8[i + 4]));
      const float a1 = (lane & 2) ? ((lane & 1) ? a4[3] : a4[2]) : ((lane & 1) ? a4[1] : a4[0]);
      float sm, sq;
      sw32_split(a1, sm, sq);
      const float mean = sm * (1.f / 512.f);
      const float var = fmaxf(sq * (1.f / 512.f) - mean * mean, 0.f);
      const float r1 = __builtin_amdgcn_rsqf(var + 1e-5f);
      const float sc = r1 * __builtin_amdgcn_rsqf(var * r1 * r1 + 1e-5f), of = -mean * sc;
      v4u hv[8];
#pragma unroll
      for (int i = 0; i < 8; ++i) {
        const int sl = (i & 3) + 16 * (i >> 2);
        const float s1 = uf(__builtin_amdgcn_readlane(fu(sc), sl)), o1 = uf(__builtin_amdgcn_readlane(fu(of), sl));
        const f32x2 s2 = {s1, s1}, o2 = {o1, o1};
#pragma unroll
        for (int e = 0; e < 4; ++e) hv[i][e] = pk2(__builtin_elementwise_fma(v[i][e], s2, o2));
      }
#pragma unroll
      for (int ps = 0; ps < 2; ++ps) {
        if (hh == ps) {
#pragma unroll
          for (int i = 0; i < 8; ++i) lds_w128(sg + i * 512 + ((r * 16) ^ ((i & 3) << 6)), hv[i]);
        }
        MEMFENCE();
#pragma unroll
        for (int i = 0; i < 4; ++i) {
          const unsigned a = sg + q * 512 + ((128 * i + 64 * hh + 32 * blk + 8 * pp) ^ (q << 6));
          st_sc1(trfrag(a, a + 2048), rs, hh * 1024 + wl * 512 + r * 16, WS_H + ((4 * ps + i) * 128 + jb) * 2048);
        }
        MEMFENCE();
      }
      {
        unsigned a, bb, c;
        split3(-mean, a, bb, c);
        if ((lane & 44) == 0) st_sc1(v4u{a | (bb << 16), a | (c << 16), bb | (a << 16), fu(r1)}, rs, ((lane & 3) + 4 * blk) * 16, WS_ST + (16 * jb + 8 * wl) * 16);
      }
      wait_vm<0>();
      if (lds_inc_rtn(W_LN) == 1) flag_store(p.ws, WS_HF + jb * 8, tag, lane);
      __builtin_amdgcn_s_setprio(0);
      TS(2);
    } else {
      const bool csum = w < 4 && ftl == 3 && (i16 & 3) < 3;
      v4u cw[16];
      if (csum) {
        const char* src = p.wqkv + (size_t)(128 * w + (lane >> 3)) * 3072 + (i16 & 3) * 1024 + (4 * gx + sx) * 128 + (lane & 7) * 16;
#pragma unroll
        for (int i = 0; i < 16; ++i) cw[i] = *(const v4u*)(src + i * 8 * 3072);
      }
      bar();
      if (csum) {
        const u64 tg = mix64(ld_epoch(p.ws));
        f32x2 a[4];
#pragma unroll
        for (int i = 0; i < 16; ++i)
#pragma unroll
          for (int e = 0; e < 4; ++e) { const f32x2 t = {bflo(cw[i][e]), bfhi(cw[i][e])}; a[e] = i ? a[e] + t : t; }
        const float c0 = dpp_add<0x128>(sw16_add(sw32_add(a[0].x, a[2].x), sw32_add(a[1].x, a[3].x)));
        const float c1 = dpp_add<0x128>(sw16_add(sw32_add(a[0].y, a[2].y), sw32_add(a[1].y, a[3].y)));
        lds_w64(X_CS + w * 256 + (8 * (lane & 7) + 4 * hh + 2 * blk) * 4, v2u{fu(c0), fu(c1)});
        if (lds_inc_rtn(W_CS) == 3) {
          const unsigned a0 = X_CS + lane * 4;
          unsigned x, y, z;
          split3((lds_r32(a0) + lds_r32(a0 + 256)) + (lds_r32(a0 + 512) + lds_r32(a0 + 768)), x, y, z);
          st_sc1(v4u{x | (x << 16), y | (x << 16), y | (z << 16), 0u}, rs, lane * 16, WS_CS + ((i16 & 3) * 512 + 64 * (4 * gx + sx)) * 16);
          wait_vm<0>();
          flag_store(p.ws, WS_CF + (4 * (4 * gx + sx) + (i16 & 3)) * 8, tg, lane);
        }
      }
    }
    lds_wait<1>(LFw, 1);
#pragma unroll
    for (int i = 0; i < 8; ++i) {
      const unsigned a = (i < 6 ? 2 * STG + i * 2048 : STG - 4096 + i * 2048) + wh * 12288 + (tl3 ^ (wl << 6));
      wof[i] = trfrag(a, a + 1024);
    }
    lds_inc(FRw);
    {
      const unsigned tb = (8 * hh + q) * 192 + 32 * blk + 8 * pp + wh * 12288;
      const unsigned ta[3] = {opaque(tb), opaque(tb + STG), opaque(tb + 2 * STG)};
      v4u wr[33][3], hr[32];
      auto rd = [&](int i, int fb) {
        const unsigned a = ta[(i >> 2) % 3] + (i & 3) * 3072 + 64 * fb;
        wr[i][fb] = trfrag(a, a + 768);
      };
      auto ldh = [&](int i) {
        hr[i] = ld_sc1(rs, lane * 16 + (i & 1) * 2048, WS_H + (dt * 128 + 32 * wh + 4 * (((i >> 2) + rotb) & 7) + (i & 2)) * 2048 + wl * 1024);
      };
      f32x16 acc[3] = {zero, zero, zero};
      if (wh == 0) {
#pragma unroll
        for (int fb = 0; fb < 3; ++fb)
#pragma unroll
          for (int qd = 0; qd < 4; ++qd) {
            const v2u bv = *(const v2u*)(p.b1 + (f0 + 32 * fb + 8 * qd + 4 * hh) * 2);
            acc[fb][4 * qd] = bflo(bv.x); acc[fb][4 * qd + 1] = bfhi(bv.x); acc[fb][4 * qd + 2] = bflo(bv.y); acc[fb][4 * qd + 3] = bfhi(bv.y);
          }
      }
      PIN(acc[0]); PIN(acc[1]); PIN(acc[2]);
      if (w == 7) {
#pragma unroll 1
        for (unsigned spins = 0; spins < SPIN_CAP; ++spins) {
          const v4u a = ld_sc1(rs, lane * 16, WS_HF + rep);
          const unsigned tl = (unsigned)tag, th = (unsigned)(tag >> 32);
          const bool ok = a.x == tl && a.y == th && a.z == tl && a.w == th;
          if (__all(ok)) break;
          __builtin_amdgcn_s_sleep(1);
          MEMFENCE();
        }
      }
      bar();
      TS(3);
#pragma unroll
      for (int i = 0; i < DH; ++i) ldh(i);
      rd(0, 0); rd(0, 1); rd(0, 2);
      SB();
#pragma unroll
      for (int i = 0; i < 32; ++i) {
        if ((i & 3) == 3) { BT(2 * (i >> 2)); bar(); BT(2 * (i >> 2) + 1); }
        SB();
        acc[0] = mfma(wr[i][0], hr[i], acc[0]);
        SB();
        if (i + 1 < 32) { rd(i + 1, 0); rd(i + 1, 1); rd(i + 1, 2); }
        SB();
        acc[1] = mfma(wr[i][1], hr[i], acc[1]);
        SB();
        if (i + DH < 32) ldh(i + DH);
        SB();
        acc[2] = mfma(wr[i][2], hr[i], acc[2]);
        SB();
      }
      TS(4);
      rd_w2a();
      lds_inc(FRw);
#pragma unroll
      for (int fb = 0; fb < 3; ++fb)
#pragma unroll
        for (int qd = 0; qd < 4; ++qd)
          lds_w128(w * 12288 + (fb * 4 + qd) * 1024 + lane * 16, f4(acc[fb][4 * qd], acc[fb][4 * qd + 1], acc[fb][4 * qd + 2], acc[fb][4 * qd + 3]));
      cbar(1);
      const int nht = w < 4 ? 2 : 1, ht0 = w < 4 ? 2 * w : 4 + w;
#pragma unroll
      for (int e = 0; e < 2; ++e) {
        if (e < nht) {
          const int ht = ht0 + e;
          const unsigned a = (ht & 1) * 12288 + (ht >> 1) * 2048 + lane * 16;
          float s[8];
#pragma unroll
          for (int ww = 0; ww < 4; ++ww) {
            const v4u lo = lds_r128(a + ww * 24576), hi = lds_r128(a + ww * 24576 + 1024);
#pragma unroll
            for (int j = 0; j < 4; ++j) { s[j] = ww ? s[j] + uf(lo[j]) : uf(lo[j]); s[4 + j] = ww ? s[4 + j] + uf(hi[j]) : uf(hi[j]); }
          }
          const v4u uo = {pk(gelu(s[0]), gelu(s[1])), pk(gelu(s[2]), gelu(s[3])), pk(gelu(s[4]), gelu(s[5])), pk(gelu(s[6]), gelu(s[7]))};
          if (ht >= 4 * skip) st_sc1(uo, rs, lane * 16, WS_U + ((dt * 86 + (f0 >> 4) + (ht >> 1)) * 2 + (ht & 1)) * 1024);
        }
      }
    }
    lds_inc(W_FX);
    { const v2u t = lds_r64(W_TAG); tag = ((u64)rfl(t.y) << 32) | rfl(t.x); }
    publish(p.ws, WS_UF + (dt * 16 + 4 * sx + ftl) * 8, tag, lane);
    TS(6);
    ngo = 0;
  }

  {
    const int ks0 = 22 * wh, nu = wh < 3 ? 22 : 20;
    const int srow = 64 * st64 + (tid >> 3);
    const v4u xres = *(const v4u*)(p.x + (size_t)srow * 1024 + dt3 * 128 + (tid & 7) * 16);
    const float b2v = bflo(*(const unsigned short*)(p.b2 + srow * 2));
    const unsigned t0[2] = {opaque(ga + tb3), opaque(ga + (tb3 ^ 64))};
    const unsigned t1[2] = {opaque(tb3), opaque(tb3 ^ 64)};
    const unsigned t2[2] = {opaque(gc + tb3), opaque(gc + (tb3 ^ 64))};
    v4u ar[8][2], ur[22];
    auto rd = [&](int i, int c) {
      const unsigned a = i < 7 ? t0[c] + i * ROW : i < 10 ? t0[c] + (i - 7) * ROW : i < 18 ? t1[c] + (i - 10) * ROW : t2[c] + (i - 18) * ROW;
      wr3[i][c] = trfrag(a, a + 1024);
    };
    TS(7);
    gate(p.ws, WS_OF + rep + ((st64 >> 1) * 8 + (lane & 7)) * 8, tag, w, ngo + 1);
    TS(8);
    f32x16 acc[2] = {zero, zero};
#pragma unroll
    for (int hf = 0; hf < 2; ++hf) {
#pragma unroll
      for (int i = 4 * hf; i < 4 * hf + 4; ++i) {
        const unsigned so = WS_O + (st64 * 32 + 8 * wh + i) * 2048;
        ar[i][0] = ld_sc1(rs, lane * 16, so); ar[i][1] = ld_sc1(rs, lane * 16 + 1024, so);
      }
      SB();
    }
#pragma unroll
    for (int i = 0; i < 8; ++i) {
      acc[0] = mfma(wof[i], ar[i][0], acc[0]);
      acc[1] = mfma(wof[i], ar[i][1], acc[1]);
      SB();
    }
    gate(p.ws, WS_UF + rep + (dt3 * 16 + (lane & 15)) * 8, tag, w, ngo + 2);
    TS(9);
    auto ldu = [&](int k) {
      const int kk = k < nu ? k : nu - 1;
      ur[k] = ld_sc1(rs, lane * 16, WS_U + (dt3 * 86 + ks0 + kk) * 2048 + wl * 1024);
    };
#pragma unroll
    for (int k = 0; k < DU; ++k) ldu(k);
    lds_wait(LFw, 2);
    SB();
#pragma unroll
    for (int k = 0; k < 22; ++k) {
      if (k == 7) lds_wait(LFw, 3);
      if (k < 20 || wh < 3) {
        acc[0] = mfma(ur[k], wr3[k][0], acc[0]);
        SB();
        if (k + 3 < 22) { rd(k + 3, 0); rd(k + 3, 1); }
        SB();
        acc[1] = mfma(ur[k], wr3[k][1], acc[1]);
        SB();
        if (k + DU < 22) ldu(k + DU);
        SB();
      }
    }
    TS(10);
    bar();
#pragma unroll
    for (int c = 0; c < 2; ++c)
#pragma unroll
      for (int qd = 0; qd < 4; ++qd)
        lds_w128(wh * 17408 + (32 * c + r) * 272 + (32 * wl + 8 * qd + 4 * hh) * 4, f4(acc[c][4 * qd], acc[c][4 * qd + 1], acc[c][4 * qd + 2], acc[c][4 * qd + 3]));
    bar();
    {
      float s[8];
#pragma unroll
      for (int e = 0; e < 4; ++e) { s[2 * e] = bflo(xres[e]) + b2v; s[2 * e + 1] = bfhi(xres[e]) + b2v; }
#pragma unroll
      for (int ww = 0; ww < 4; ++ww) {
        const unsigned a = ww * 17408 + (tid >> 3) * 272 + (tid & 7) * 32;
        const v4u lo = lds_r128(a), hi = lds_r128(a + 16);
#pragma unroll
        for (int j = 0; j < 4; ++j) { s[j] += uf(lo[j]); s[4 + j] += uf(hi[j]); }
      }
      *(v4u*)(p.out + (size_t)srow * 1024 + dt3 * 128 + (tid & 7) * 16) = v4u{pk(s[0], s[1]), pk(s[2], s[3]), pk(s[4], s[5]), pk(s[6], s[7])};
    }
    if (b == 0 && tid == 448) *(u64*)p.ws = tag;
  }
}
}

extern "C" void kernel_launch(void* const* d_in, const int* in_sizes, int n_in, void* d_out, int out_size, void* d_ws,
                              size_t ws_size, hipStream_t stream) {
  hk::Params p;
  p.x = (const char*)d_in[0]; p.wqkv = (const char*)d_in[1]; p.wphi = (const char*)d_in[2]; p.wo = (const char*)d_in[3];
  p.w1 = (const char*)d_in[5]; p.b1 = (const char*)d_in[6]; p.w2 = (const char*)d_in[7]; p.b2 = (const char*)d_in[8];
  p.out = (char*)d_out; p.ws = (char*)d_ws;
  (void)hipFuncSetAttribute((const void*)hk::fused, hipFuncAttributeMaxDynamicSharedMemorySize, hk::LDS_BYTES);
  hk::fused<<<256, 768, hk::LDS_BYTES, stream>>>(p);
}
```

```cpp
#include <hip/hip_runtime.h>
#include <stdint.h>

#define GAS __attribute__((address_space(1)))
#define LAS __attribute__((address_space(3)))
#define DI __device__ __forceinline__
typedef short bf16x8 __attribute__((ext_vector_type(8)));
typedef short s16x4 __attribute__((ext_vector_type(4)));
typedef __bf16 bf2 __attribute__((ext_vector_type(2)));
typedef float f32x16 __attribute__((ext_vector_type(16)));
typedef float f32x2 __attribute__((ext_vector_type(2)));
typedef unsigned v4u __attribute__((ext_vector_type(4)));
typedef unsigned v2u __attribute__((ext_vector_type(2)));
typedef unsigned long long u64;
typedef GAS u64 gu64;

namespace hk {
constexpr unsigned REPB = 8192;
constexpr unsigned WS_HF = 4096, WS_KF = WS_HF + 1024, WS_OF = WS_KF + 1024, WS_UF = WS_OF + 1024;
constexpr unsigned WS_ST = 98304, WS_ZP = 131072, WS_KVP = 196608, WS_CS = 720896, WS_TS = 800 * 1024;
constexpr unsigned WS_H = 1u << 20, WS_U = 3u << 20, WS_O = 9u << 19, WS_BYTES = 13u << 19;
constexpr unsigned REG = 40960, ROW = 8192, STG = 49152, LDS_BYTES = 163840;
constexpr unsigned SPIN_CAP = 1u << 14;
constexpr int AUX_SC1 = 16;
constexpr float LOG2E = 1.4426950408889634f;

struct Params {
  const char *x, *wqkv, *wphi, *wo, *w1, *b1, *w2, *b2;
  char *out, *ws;
};

DI f32x16 mfma(v4u a, v4u b, f32x16 c) {
  return __builtin_amdgcn_mfma_f32_32x32x16_bf16(__builtin_bit_cast(bf16x8, a), __builtin_bit_cast(bf16x8, b), c, 0, 0, 0);
}
DI unsigned pk(float a, float b) { bf2 v = {(__bf16)a, (__bf16)b}; return __builtin_bit_cast(unsigned, v); }
DI unsigned pk2(f32x2 a) { return pk(a.x, a.y); }
DI float bflo(unsigned u) { return __uint_as_float(u << 16); }
DI float bfhi(unsigned u) { return __uint_as_float(u & 0xffff0000u); }
DI float uf(unsigned u) { return __uint_as_float(u); }
DI unsigned fu(float f) { return __float_as_uint(f); }
template <int O> DI v4u pack8(const f32x16& x) {
  return v4u{pk(x[O], x[O + 1]), pk(x[O + 2], x[O + 3]), pk(x[O + 4], x[O + 5]), pk(x[O + 6], x[O + 7])};
}
DI v4u f4(float a, float b, float c, float d) { return v4u{fu(a), fu(b), fu(c), fu(d)}; }
DI v2u trr(unsigned a) { return __builtin_bit_cast(v2u, __builtin_amdgcn_ds_read_tr16_b64_v4i16((LAS s16x4*)(size_t)a)); }
DI v4u trfrag(unsigned a0, unsigned a1) { v2u lo = trr(a0), hi = trr(a1); return v4u{lo.x, lo.y, hi.x, hi.y}; }
DI v4u lds_r128(unsigned a) { return *(LAS v4u*)(size_t)a; }
DI v2u lds_r64(unsigned a) { return *(LAS v2u*)(size_t)a; }
DI float lds_r32(unsigned a) { return *(LAS float*)(size_t)a; }
DI void lds_w128(unsigned a, v4u v) { *(LAS v4u*)(size_t)a = v; }
DI void lds_w64(unsigned a, v2u v) { *(LAS v2u*)(size_t)a = v; }
DI void lds_w32(unsigned a, float v) { *(LAS float*)(size_t)a = v; }
DI void glds1(unsigned lds, const char* sb, unsigned v0) {
  asm volatile("s_mov_b32 m0, %0\n\ts_nop 0\n\tglobal_load_lds_dwordx4 %2, %1" ::"s"(lds), "s"(sb), "v"(v0) : "memory");
}
DI void glds2(unsigned lds, const char* sb, unsigned v0, unsigned v1) {
  asm volatile("s_mov_b32 m0, %0\n\ts_nop 0\n\tglobal_load_lds_dwordx4 %2, %1\n\tglobal_load_lds_dwordx4 %3, %1 offset:1024"
               ::"s"(lds), "s"(sb), "v"(v0), "v"(v1) : "memory");
}
DI void glds3(unsigned lds, const char* sb, unsigned v0, unsigned v1, unsigned v2) {
  asm volatile("s_mov_b32 m0, %0\n\ts_nop 0\n\tglobal_load_lds_dwordx4 %2, %1\n\tglobal_load_lds_dwordx4 %3, %1 offset:1024\n\t"
               "global_load_lds_dwordx4 %4, %1 offset:2048"
               ::"s"(lds), "s"(sb), "v"(v0), "v"(v1), "v"(v2) : "memory");
}
#define SB() __builtin_amdgcn_sched_barrier(0)
#define PIN(x) asm volatile("" : "+v"(x))
#define MEMFENCE() asm volatile("" ::: "memory")
DI unsigned opaque(unsigned v) { asm volatile("" : "+v"(v)); return v & 0x3ffffu; }
DI unsigned rfl(unsigned v) { return __builtin_amdgcn_readfirstlane(v); }
template <int N> DI void wait_vm() { asm volatile("s_waitcnt vmcnt(%0)" ::"n"(N) : "memory"); }
DI void wait_lgkm0() { asm volatile("s_waitcnt lgkmcnt(0)" ::: "memory"); }
DI void bar() { asm volatile("s_waitcnt lgkmcnt(0)" ::: "memory"); __builtin_amdgcn_s_barrier(); MEMFENCE(); }
DI v4u ld_sc1(__amdgpu_buffer_rsrc_t rs, unsigned voff, unsigned soff) { return __builtin_amdgcn_raw_buffer_load_b128(rs, voff, soff, AUX_SC1); }
DI void st_sc1(v4u v, __amdgpu_buffer_rsrc_t rs, unsigned voff, unsigned soff) { __builtin_amdgcn_raw_buffer_store_b128(v, rs, voff, soff, AUX_SC1); }
template <int OFF> DI void ald_sc1(v4u& d, __amdgpu_buffer_rsrc_t rs, unsigned voff, unsigned soff) {
  asm volatile("buffer_load_dwordx4 %0, %1, %2, %3 offen offset:%4 sc1" : "=v"(d) : "v"(voff), "s"(rs), "s"(soff), "n"(OFF) : "memory");
}
template <int N> DI void use_vm(v4u& d) { asm volatile("s_waitcnt vmcnt(%1)" : "+v"(d) : "n"(N) : "memory"); }

DI void publish(char* ws, unsigned off, u64 tag, int tid) {
  wait_vm<0>();
  bar();
  if ((tid >> 3) == 56) __hip_atomic_store((gu64*)(ws + off + (tid & 7) * REPB), tag, __ATOMIC_RELAXED, __HIP_MEMORY_SCOPE_AGENT);
  MEMFENCE();
}
DI void poll(char* ws, unsigned off, u64 tag) {
  gu64* p = (gu64*)(ws + off);
#pragma unroll 1
  for (unsigned spins = 0; spins < SPIN_CAP; ++spins) {
    u64 v = __hip_atomic_load(p, __ATOMIC_RELAXED, __HIP_MEMORY_SCOPE_AGENT);
    if (__all(v == tag)) break;
    __builtin_amdgcn_s_sleep(1);
  }
  __builtin_amdgcn_fence(__ATOMIC_ACQUIRE, "wavefront");
  MEMFENCE();
}

template <int CTRL> DI float dpp_add(float v) { return v + uf(__builtin_amdgcn_update_dpp(0u, fu(v), CTRL, 0xf, 0xf, true)); }
DI float row_sum(float v) { return dpp_add<0x140>(dpp_add<0x141>(dpp_add<0x4E>(dpp_add<0xB1>(v)))); }
DI float sw16_add(float a, float b) { auto r = __builtin_amdgcn_permlane16_swap(fu(a), fu(b), false, false); return uf(r[0]) + uf(r[1]); }
DI float sw32_add(float a, float b) { auto r = __builtin_amdgcn_permlane32_swap(fu(a), fu(b), false, false); return uf(r[0]) + uf(r[1]); }
DI void sw32_split(float m, float& a, float& b) { auto q = __builtin_amdgcn_permlane32_swap(fu(m), fu(m), false, false); a = uf(q[0]); b = uf(q[1]); }
DI float mx(float a, float b) { return __builtin_elementwise_maximum(a, b); }
DI float mn(float a, float b) { return __builtin_elementwise_minimum(a, b); }
template <int O> DI void hh_exp(const f32x16& P, float c, f32x2 (&ep)[4], f32x2 (&en)[4]) {
  const float hi = mx(mx(mx(P[O], P[O + 1]), P[O + 2]), mx(mx(mx(P[O + 3], P[O + 4]), P[O + 5]), mx(P[O + 6], P[O + 7])));
  const float lo = mn(mn(mn(P[O], P[O + 1]), P[O + 2]), mn(mn(mn(P[O + 3], P[O + 4]), P[O + 5]), mn(P[O + 6], P[O + 7])));
  auto r = __builtin_amdgcn_permlane32_swap(fu(hi), fu(-lo), false, false);
  float bp, bn;
  sw32_split(mx(uf(r[0]), uf(r[1])) * -c, bp, bn);
  const f32x2 c2 = {c, c}, bp2 = {bp, bp}, bn2 = {bn, bn};
#pragma unroll
  for (int j = 0; j < 4; ++j) {
    const f32x2 pv = {P[O + 2 * j], P[O + 2 * j + 1]};
    const f32x2 a = __builtin_elementwise_fma(pv, c2, bp2), b = __builtin_elementwise_fma(pv, -c2, bn2);
    ep[j] = f32x2{__builtin_amdgcn_exp2f(a.x), __builtin_amdgcn_exp2f(a.y)};
    en[j] = f32x2{__builtin_amdgcn_exp2f(b.x), __builtin_amdgcn_exp2f(b.y)};
  }
}
template <int O> DI void hedgehog(const f32x16& P, float c, v4u& fp, v4u& fn) {
  f32x2 ep[4], en[4];
  hh_exp<O>(P, c, ep, en);
  const f32x2 s2 = (ep[0] + ep[1]) + (ep[2] + ep[3]), t2 = (en[0] + en[1]) + (en[2] + en[3]);
  float rp, rn;
  sw32_split(__builtin_amdgcn_rcpf(sw32_add(s2.x + s2.y, t2.x + t2.y)), rp, rn);
  const f32x2 rp2 = {rp, rp}, rn2 = {rn, rn};
  fp = v4u{pk2(ep[0] * rp2), pk2(ep[1] * rp2), pk2(ep[2] * rp2), pk2(ep[3] * rp2)};
  fn = v4u{pk2(en[0] * rn2), pk2(en[1] * rn2), pk2(en[2] * rn2), pk2(en[3] * rn2)};
}
DI unsigned split2(float v) {
  const unsigned a = pk(v, 0.f);
  return pk(v, v - bflo(a));
}
DI float gelu(float x) {
  const float e = x * __builtin_fmaf(x * x, -2.f * LOG2E * 0.044715f * 0.7978845608028654f, -2.f * LOG2E * 0.7978845608028654f);
  return x * __builtin_amdgcn_rcpf(1.f + __builtin_amdgcn_exp2f(e));
}
DI u64 ld_epoch(const char* ws) {
  u64 e;
  asm volatile("s_load_dwordx2 %0, %1, 0x0\n\ts_waitcnt lgkmcnt(0)" : "=s"(e) : "s"(ws) : "memory");
  return e;
}
DI u64 mix64(u64 z) {
  z += 0x9E3779B97F4A7C15ull; z = (z ^ (z >> 30)) * 0xBF58476D1CE4E5B9ull; z = (z ^ (z >> 27)) * 0x94D049BB133111EBull;
  return z ^ (z >> 31);
}

#define BT(k)
#define CB(v)
#define CE(v, a)
#define TS(k) do { } while (0)

constexpr int DH = 8;
constexpr int OGATE = 12;
constexpr int b_g(int i) { return (i == 4 || i == 5 || i == 8 || i == 9) ? 3 : i >= 24 ? 1 : 0; }
constexpr int b_h(int i) { return i < DH - 4 ? 2 : i + DH < 32 ? 1 : 0; }
constexpr int b_hfirst(int i) { return i < DH - 4 ? 4 + 2 * i : i + DH; }
constexpr int b_wait_h(int i) {
  int n = 0; bool seen = false;
  for (int j = 0; j < 4; ++j) { if (seen) ++n; if (j == i) seen = true; }
  for (int s = 0; s < i; ++s) {
    if (seen) n += b_g(s);
    for (int j = 0; j < b_h(s); ++j) { if (seen) ++n; if (b_hfirst(s) + j == i) seen = true; }
  }
  return n;
}
constexpr int b_wait_g(int from, int at) {
  int n = b_h(from);
  for (int s = from + 1; s < at; ++s) n += b_g(s) + b_h(s);
  return n;
}
template <int I> struct IC { static constexpr int v = I; };
template <int LO, int HI, class F> DI void sfor(F&& f) { if constexpr (LO < HI) { f(IC<LO>{}); sfor<LO + 1, HI>(f); } }

struct P3Src { const char* wo; const char* w2; unsigned vo, v2; int kmax; unsigned b0, b1, b2; };
DI void p3_issue(const P3Src& s, int i, int w) {
  const int j = i < 20 ? i : i - 20;
  const unsigned lds = rfl((j < 8 ? s.b0 + j * ROW : j < 15 ? s.b1 + (j - 8) * ROW : s.b2 + (j - 15) * ROW) + w * 1024);
  if (i < 8) glds1(lds, s.wo + i * 16 * 1024, s.vo);
  else { int k = i - 8; k = k < s.kmax ? k : s.kmax; glds1(lds, s.w2 + (size_t)k * 65536, s.v2); }
}

__global__ __launch_bounds__(512, 2) void fused(Params p) {
  extern __shared__ __attribute__((aligned(16))) char smem[];
  const int tid = threadIdx.x, lane = tid & 63, w = rfl(tid >> 6);
  const int r = lane & 31, hh = lane >> 5, blk = (lane >> 4) & 1, q = (lane >> 2) & 3, pp = lane & 3;
  const int b = blockIdx.x, xg = b & 7, idx = b >> 3, sx = xg >> 1, gx = xg & 1, i16 = idx & 15;
  const int wh = w >> 1, wl = w & 1;
  const bool roleA = idx < 16;
  TS(0);
  u64 tag;
  const __amdgpu_buffer_rsrc_t rs = __builtin_amdgcn_make_buffer_rsrc(p.ws, 0, WS_BYTES, 0x00020000);
  const unsigned rep = xg * REPB;
  asm volatile("" ::"v"((LAS char*)smem));
  f32x16 zero;
#pragma unroll
  for (int e = 0; e < 16; ++e) zero[e] = 0.f;

  const int st64 = 8 * sx + (i16 & 7);
  const int dt3 = 4 * gx + (((i16 >> 3) + 2 * (i16 & 1) + 2 * (idx >> 4)) & 3);
  const unsigned ch4 = ((lane & 7) ^ (((lane >> 4) & 1) << 2)) * 16;
  P3Src s3;
  {
    const unsigned row = 8 * wl + (lane >> 3);
    s3.wo = p.wo + (size_t)(128 * wh) * 1024 + dt3 * 128;
    s3.w2 = p.w2 + (size_t)(22 * wh) * 65536 + st64 * 128;
    s3.vo = row * 1024 + ch4;
    s3.v2 = row * 4096 + ch4;
    s3.kmax = wh < 3 ? 21 : 19;
    s3.b0 = roleA ? 0 : 12 * ROW; s3.b1 = roleA ? 8 * ROW : 0; s3.b2 = roleA ? 10 * ROW : 7 * ROW;
  }
  const unsigned tb3 = wh * 2048 + (4 * hh + q) * 128 + ((32 * blk + 8 * pp) ^ ((q >> 1) << 6));
  v4u wof[8], wr3[30][2];
  auto rd_wo = [&]() {
    const unsigned tw = opaque(s3.b0 + (tb3 ^ (wl << 6)));
#pragma unroll
    for (int i = 0; i < 8; ++i) wof[i] = trfrag(tw + i * ROW, tw + i * ROW + 1024);
  };
  auto rd_89 = [&]() {
#pragma unroll
    for (int i = 8; i < 15; ++i)
#pragma unroll
      for (int c = 0; c < 2; ++c) { const unsigned a = s3.b1 + (i - 8) * ROW + (tb3 ^ (c << 6)); wr3[i][c] = trfrag(a, a + 1024); }
  };

  if (roleA) {
    const int st = 4 * sx + (idx & 3), g = 4 * gx + (idx >> 2), rg = wh, cg = wl, pair = 2 * g + cg;
    const unsigned xc = ((lane & 7) ^ (lane >> 4)) * 16, xr = (lane >> 3) * 1024;
    const unsigned vx0 = xr + xc, vx1 = xr + (xc ^ 64) + 8192;
    const unsigned vw0 = (lane >> 3) * 3072 + ch4;
    const char* xs = p.x + (size_t)(128 * st + 16 * w) * 1024;
    const char* wsrc = p.wqkv + (size_t)(8 * w) * 3072 + g * 128;
    auto issue = [&](int c, int n) {
      const unsigned sl = (c & 3) * REG;
      if (n < 2) glds1(sl + w * 2048 + n * 1024, xs + c * 128, n ? vx1 : vx0);
      else glds1(sl + 16384 + (n - 2) * 8192 + w * 1024, wsrc + (size_t)c * 64 * 3072 + (n - 2) * 1024, vw0);
    };
#pragma unroll
    for (int c = 0; c < 2; ++c)
#pragma unroll
      for (int n = 0; n < 5; ++n) issue(c, n);
    v4u wph;
    asm volatile("global_load_dwordx4 %0, %1, %2" : "=v"(wph) : "v"(lane * 16), "s"(p.wphi + pair * 1024) : "memory");
    v4u id0, id1;
    {
      const v4u z4 = {0, 0, 0, 0};
      const int rr = (r & 15) - 4 * hh;
      v4u e1;
      e1.x = rr == 0 ? 0x3f80u : rr == 1 ? 0x3f800000u : 0u; e1.y = rr == 2 ? 0x3f80u : rr == 3 ? 0x3f800000u : 0u;
      e1.z = rr == 8 ? 0x3f80u : rr == 9 ? 0x3f800000u : 0u; e1.w = rr == 10 ? 0x3f80u : rr == 11 ? 0x3f800000u : 0u;
      id0 = r < 16 ? e1 : z4; id1 = r < 16 ? z4 : e1;
      PIN(id0); PIN(id1);
    }
    tag = mix64(ld_epoch(p.ws));
    f32x16 acc[3] = {zero, zero, zero};
    unsigned xa[2][4], wa[4];
#pragma unroll
    for (int k = 0; k < 4; ++k) {
      xa[0][k] = opaque((32 * rg + r) * 128 + (unsigned)(((2 * k + hh) ^ ((r >> 1) & 7)) << 4)); xa[1][k] = opaque(xa[0][k] + 2 * REG);
      wa[k] = opaque(k * REG + 16384 + (8 * hh + q) * 128 + ((64 * cg + 32 * blk + 8 * pp) ^ ((q >> 1) << 6)));
    }
    v4u fx[8][4], fw[8][4][3], stv, cg3[3];
    auto rdk = [&](int c, int k, int t) {
      if (t == 0) fx[c][k] = lds_r128(xa[(c >> 1) & 1][k] + (c & 1) * REG);
      const unsigned a = wa[c & 3] + k * 2048 + t * 8192;
      fw[c][k][t] = trfrag(a, a + 512);
    };
    auto ld_stat = [&](int t) {
      if (t == 3) ald_sc1<0>(stv, rs, r * 16, WS_ST + (128 * st + 32 * rg) * 16);
      else ald_sc1<0>(cg3[t], rs, r * 32 + hh * 16, WS_CS + (512 * t + 32 * pair) * 32);
    };
    auto feed = [&](int c) {
#pragma unroll
      for (int n = 0; n < 5; ++n) { if (c < 5) issue(c + 3, n); else p3_issue(s3, 5 * (c - 5) + n, w); }
    };
    PIN(acc[0]); PIN(acc[1]); PIN(acc[2]);
    wait_vm<6>();
    bar();
    TS(1);
#pragma unroll
    for (int k = 0; k < 4; ++k) { rdk(0, k, 0); rdk(0, k, 1); rdk(0, k, 2); }
    feed(-1);
#pragma unroll
    for (int c = 0; c < 8; ++c) {
      if (c < 6) wait_vm<5>();
      if (c == 6) wait_vm<9>();
      bar();
      BT(c);
      if (c == 7) { PIN(wph); lds_w128(15 * ROW + w * 4096 + lane * 16, wph); }
      if (w >= 4) feed(c);
#pragma unroll
      for (int k = 0; k < 4; ++k)
#pragma unroll
        for (int t = 0; t < 3; ++t) {
          SB();
          acc[t] = mfma(fw[c][k][t], fx[c][k], acc[t]);
          SB();
          if (c < 7) rdk(c + 1, k, t);
          if (c == 5 && t == 1) ld_stat(k);
          if (c == 7 && k == 1 && t == 0) { const unsigned a = 15 * ROW + w * 4096 + blk * 512 + (4 * hh + q) * 32 + 8 * pp; wph = trfrag(a, a + 256); }
        }
      SB();
      if (w < 4) feed(c);
    }
    TS(2);
    {
      const unsigned tl = (unsigned)tag;
      asm volatile("s_waitcnt vmcnt(10)" : "+v"(stv), "+v"(cg3[0]), "+v"(cg3[1]), "+v"(cg3[2])::"memory");
#pragma unroll 1
      for (unsigned spins = 0; spins < SPIN_CAP; ++spins) {
        const bool ok = stv.y == tl && stv.w == tl && cg3[0].y == tl && cg3[0].w == tl && cg3[1].y == tl && cg3[1].w == tl && cg3[2].y == tl && cg3[2].w == tl;
        if (__all(ok)) break;
        ld_stat(0); ld_stat(1); ld_stat(2); ld_stat(3);
        asm volatile("s_waitcnt vmcnt(0)" : "+v"(stv), "+v"(cg3[0]), "+v"(cg3[1]), "+v"(cg3[2])::"memory");
      }
    }
    BT(8);
    v4u aphi0, aphi1;
    {
      const v4u z4 = {0, 0, 0, 0};
      aphi0 = r < 16 ? wph : z4; aphi1 = r < 16 ? z4 : wph;
    }
    const unsigned EX = 15 * ROW, X_Z = 19 * ROW;
    const float rstd = uf(stv.z);
    {
      const v4u mf = {stv.x, stv.x, stv.x, stv.x};
#pragma unroll
      for (int t = 1; t < 4; ++t) {
        const v4u c = cg3[t % 3];
        const v4u cf = {__builtin_amdgcn_perm(c.x, c.x, 0x01000100u), __builtin_amdgcn_perm(c.x, c.x, 0x03020302u),
                        __builtin_amdgcn_perm(c.z, c.z, 0x01000100u), __builtin_amdgcn_perm(c.z, c.z, 0x03020302u)};
        acc[t % 3] = mfma(cf, mf, acc[t % 3]);
      }
    }
    const float cexp = rstd * LOG2E;
    {
      f32x16 P = mfma(aphi0, pack8<0>(acc[1]), zero);
      P = mfma(aphi1, pack8<8>(acc[1]), P);
      f32x16 vs;
#pragma unroll
      for (int e = 0; e < 16; ++e) vs[e] = acc[2][e] * rstd;
      f32x16 zv = mfma(pack8<0>(vs), id0, zero);
      zv = mfma(pack8<8>(vs), id1, zv);
      v4u fp, fn, gp, gn;
      hedgehog<0>(P, cexp, fp, fn);
      hedgehog<8>(P, cexp, gp, gn);
      f32x16 zf = mfma(fp, id0, zero), zg = mfma(gp, id0, zero);
      zf = mfma(fn, id1, zf); zg = mfma(gn, id1, zg);
      const v4u vt0 = pack8<0>(zv), vt1 = pack8<8>(zv);
      f32x16 kv0 = mfma(pack8<0>(zf), vt0, zero), kv1 = mfma(pack8<0>(zg), vt0, zero);
      kv0 = mfma(pack8<8>(zf), vt1, kv0); kv1 = mfma(pack8<8>(zg), vt1, kv1);
      f32x2 z0 = {zf[0], zf[1]}, z1 = {zg[0], zg[1]};
#pragma unroll
      for (int e = 2; e < 16; e += 2) { z0 += f32x2{zf[e], zf[e + 1]}; z1 += f32x2{zg[e], zg[e + 1]}; }
      lds_w32(X_Z + w * 256 + lane * 4, sw32_add(z0.x + z0.y, z1.x + z1.y));
#pragma unroll
      for (int e = 0; e < 16; ++e) kv0[e] = r < 16 ? kv0[e] : kv1[e];
#pragma unroll
      for (int i = 0; i < 4; ++i) lds_w128(EX + w * 4096 + i * 1024 + lane * 16, f4(kv0[4 * i], kv0[4 * i + 1], kv0[4 * i + 2], kv0[4 * i + 3]));
      BT(9);
      wait_vm<0>();
      bar();
      BT(10);
      if (rg < 2) {
        float s[8];
#pragma unroll
        for (int i = 0; i < 4; ++i) {
          const unsigned a = EX + (2 * i + cg) * 4096 + rg * 2048 + lane * 16;
          const v4u lo = lds_r128(a), hi = lds_r128(a + 1024);
#pragma unroll
          for (int e = 0; e < 4; ++e) { s[e] = i ? s[e] + uf(lo[e]) : uf(lo[e]); s[4 + e] = i ? s[4 + e] + uf(hi[e]) : uf(hi[e]); }
        }
        st_sc1(v4u{pk(s[0], s[1]), pk(s[2], s[3]), pk(s[4], s[5]), pk(s[6], s[7])}, rs, lane * 16, WS_KVP + ((pair * 16 + st) * 2 + rg) * 1024);
      } else if (rg == 2) {
        const unsigned a = X_Z + cg * 256 + lane * 4;
        const float zt = (lds_r32(a) + lds_r32(a + 512)) + (lds_r32(a + 1024) + lds_r32(a + 1536));
        __builtin_amdgcn_raw_buffer_store_b32(fu(zt), rs, lane * 4, WS_ZP + (pair * 16 + st) * 256, AUX_SC1);
      }
    }
    rd_wo(); rd_89();
    BT(11);
    publish(p.ws, WS_KF + (g * 16 + st) * 8, tag, tid);
    TS(3);
    if (w >= 4) {
#pragma unroll 1
      for (int i = 15; i < 30; ++i) p3_issue(s3, i, w);
    }
    v4u fq[2][2];
    {
      f32x16 P = mfma(aphi0, pack8<0>(acc[0]), zero);
      P = mfma(aphi1, pack8<8>(acc[0]), P);
      hedgehog<0>(P, cexp, fq[0][0], fq[0][1]);
      hedgehog<8>(P, cexp, fq[1][0], fq[1][1]);
      PIN(fq[0][0]); PIN(fq[0][1]); PIN(fq[1][0]); PIN(fq[1][1]);
    }
    if (w < 4) {
#pragma unroll 1
      for (int i = 15; i < 30; ++i) p3_issue(s3, i, w);
    }
    TS(4);
    if (w == 7) poll(p.ws, WS_KF + rep + (g * 16 + (lane & 15)) * 8, tag);
    bar();
    TS(5);
    {
      v4u kp[8];
#pragma unroll
      for (int i = 0; i < 8; ++i) kp[i] = ld_sc1(rs, lane * 16, WS_KVP + (pair * 32 + (rg & 1) + 2 * (8 * (rg >> 1) + i)) * 1024);
      if (rg == 0) {
        v4u zp[4];
#pragma unroll
        for (int i = 0; i < 4; ++i) zp[i] = ld_sc1(rs, lane * 16, WS_ZP + pair * 4096 + i * 1024);
        float zs[4];
#pragma unroll
        for (int e = 0; e < 4; ++e) {
          float t = (uf(zp[0][e]) + uf(zp[1][e])) + (uf(zp[2][e]) + uf(zp[3][e]));
          t = sw16_add(t, t); zs[e] = sw32_add(t, t);
        }
        lds_w64(X_Z + w * 256 + (lane & 15) * 8, v2u{pk(zs[0], zs[1]), pk(zs[2], zs[3])});
      }
      f32x16 Ssum = zero;
#pragma unroll
      for (int i = 0; i < 8; ++i) Ssum = mfma(id0, kp[i], Ssum);
      lds_w128(EX + w * 4096 + lane * 16, f4(Ssum[0], Ssum[1], Ssum[2], Ssum[3]));
      lds_w128(EX + w * 4096 + 1024 + lane * 16, f4(Ssum[4], Ssum[5], Ssum[6], Ssum[7]));
      BT(12);
      bar();
      BT(13);
      v4u ah[2][2];
#pragma unroll
      for (int t = 0; t < 2; ++t) {
        const unsigned a = EX + (2 * t + cg) * 4096 + lane * 16;
        const v4u a0 = lds_r128(a), a1 = lds_r128(a + 1024), b0 = lds_r128(a + 16384), b1 = lds_r128(a + 16384 + 1024);
        float s[8];
#pragma unroll
        for (int e = 0; e < 4; ++e) { s[e] = uf(a0[e]) + uf(b0[e]); s[4 + e] = uf(a1[e]) + uf(b1[e]); }
        const v4u m = {pk(s[0], s[1]), pk(s[2], s[3]), pk(s[4], s[5]), pk(s[6], s[7])};
#pragma unroll
        for (int h = 0; h < 2; ++h) {
          const unsigned za = X_Z + cg * 256 + (32 * h + 16 * t + 4 * hh) * 2;
          const v2u lo = lds_r64(za), hi = lds_r64(za + 16);
          const v4u zf = {lo.x, lo.y, hi.x, hi.y};
          ah[h][t] = ((r < 16) == (h == 0)) ? m : zf;
        }
      }
      const unsigned ob = WS_O + (((2 * st + (rg >> 1)) * 32 + 2 * pair) * 2 + (rg & 1)) * 1024;
      f32x16 N0 = mfma(ah[0][0], fq[0][0], zero), N1 = mfma(ah[1][0], fq[1][0], zero);
      N0 = mfma(ah[0][1], fq[0][1], N0); N1 = mfma(ah[1][1], fq[1][1], N1);
      const float r0 = __builtin_amdgcn_rcpf(N0[8] + 1e-6f), r1 = __builtin_amdgcn_rcpf(N1[0] + 1e-6f);
#pragma unroll
      for (int e = 0; e < 8; ++e) { N0[e] *= r0; N1[8 + e] *= r1; }
      st_sc1(pack8<0>(N0), rs, lane * 16, ob);
      st_sc1(pack8<8>(N1), rs, lane * 16, ob + 2048);
    }
    BT(14);
    publish(p.ws, WS_OF + (st * 8 + g) * 8, tag, tid);
    TS(6);
  } else {
    const int jb = 16 * xg + i16;
    const int ftl = i16 >> 2, dt = 4 * gx + (i16 & 3);
    const int skip = ftl < 3 ? 0 : sx < 3 ? 1 : 2;
    const int f0 = 32 * (11 * sx + 3 * ftl - skip);
    unsigned vw[3];
#pragma unroll
    for (int n = 0; n < 3; ++n) { const unsigned id = 64 * n + lane, row = id / 12, ch = id % 12; vw[n] = row * 2752 + ch * 16 - 1024 * n; }
    const int rot = 2 * ((i16 + ftl) & 3);
    const char* w1s = p.w1 + f0 * 2 + (size_t)(512 * wh + 32 * wl) * 2752;
    auto issue = [&](int s, int e) {
      glds3((s % 3) * STG + w * 6144 + e * 3072, w1s + (size_t)(64 * ((s + rot) & 7) + 16 * e) * 2752, vw[0], vw[1], vw[2]);
    };
    {
      v4u xr[2], cw[2];
      const bool csum = i16 < 12;
      {
        const char* xs = p.x + (size_t)(16 * jb + 2 * w) * 1024;
        const char* cs = p.wqkv + (size_t)(128 * (i16 & 3) + 16 * w) * 3072 + (csum ? (i16 >> 2) * 1024 : 0) + (4 * gx + sx) * 128;
        asm volatile("global_load_dwordx4 %0, %4, %5\n\tglobal_load_dwordx4 %1, %4, %5 offset:1024\n\t"
                     "global_load_dwordx4 %2, %6, %8\n\tglobal_load_dwordx4 %3, %7, %8"
                     : "=&v"(xr[0]), "=&v"(xr[1]), "=&v"(cw[0]), "=&v"(cw[1])
                     : "v"(lane * 16), "s"(xs), "v"((lane >> 3) * 3072 + (lane & 7) * 16), "v"((lane >> 3) * 3072 + (lane & 7) * 16 + 24576), "s"(cs) : "memory");
      }
      issue(0, 0); issue(0, 1);
      tag = mix64(ld_epoch(p.ws));
      asm volatile("s_waitcnt vmcnt(8)" : "+v"(xr[0]), "+v"(xr[1])::"memory");
      f32x2 v[2][4];
      float a2[2];
#pragma unroll
      for (int i = 0; i < 2; ++i) {
#pragma unroll
        for (int e = 0; e < 4; ++e) v[i][e] = f32x2{bflo(xr[i][e]), bfhi(xr[i][e])};
        const f32x2 s = (v[i][0] + v[i][1]) + (v[i][2] + v[i][3]);
        const f32x2 sq = __builtin_elementwise_fma(v[i][0], v[i][0], v[i][1] * v[i][1]) + __builtin_elementwise_fma(v[i][2], v[i][2], v[i][3] * v[i][3]);
        a2[i] = sw32_add(s.x + s.y, sq.x + sq.y);
      }
      TS(1);
      float sm, sq;
      sw32_split(row_sum(sw16_add(a2[0], a2[1])), sm, sq);
      const float mean = sm * (1.f / 512.f);
      const float var = fmaxf(sq * (1.f / 512.f) - mean * mean, 0.f);
      const float r1 = __builtin_amdgcn_rsqf(var + 1e-5f);
      const float sc = r1 * __builtin_amdgcn_rsqf(var * r1 * r1 + 1e-5f), of = -mean * sc;
      const unsigned SG = 18 * ROW, X_CS = 2 * STG;
#pragma unroll
      for (int i = 0; i < 2; ++i) {
        const float s1 = uf(__builtin_amdgcn_readlane(fu(sc), 16 * i)), o1 = uf(__builtin_amdgcn_readlane(fu(of), 16 * i));
        const f32x2 s2 = {s1, s1}, o2 = {o1, o1};
        v4u hv;
#pragma unroll
        for (int e = 0; e < 4; ++e) hv[e] = pk2(__builtin_elementwise_fma(v[i][e], s2, o2));
        const int rho = 2 * w + i;
        lds_w128(SG + rho * 1024 + ((lane * 16) ^ ((rho & 3) << 6)), hv);
      }
      bar();
#pragma unroll
      for (int i = 0; i < 2; ++i) {
        const unsigned a = SG + (8 * hh + q) * 1024 + ((128 * w + 64 * i + 32 * blk + 8 * pp) ^ (q << 6));
        st_sc1(trfrag(a, a + 4096), rs, lane * 16 + i * 1024, WS_H + (w * 128 + jb) * 2048);
      }
      if ((lane & 47) == 0) st_sc1(v4u{split2(-mean), (unsigned)tag, fu(r1), (unsigned)tag}, rs, blk * 16, WS_ST + (16 * jb + 2 * w) * 16);
      {
        asm volatile("s_waitcnt vmcnt(9)" : "+v"(cw[0]), "+v"(cw[1])::"memory");
        f32x2 a[4];
#pragma unroll
        for (int e = 0; e < 4; ++e) a[e] = f32x2{bflo(cw[0][e]), bfhi(cw[0][e])} + f32x2{bflo(cw[1][e]), bfhi(cw[1][e])};
        const float c0 = dpp_add<0x128>(sw16_add(sw32_add(a[0].x, a[2].x), sw32_add(a[1].x, a[3].x)));
        const float c1 = dpp_add<0x128>(sw16_add(sw32_add(a[0].y, a[2].y), sw32_add(a[1].y, a[3].y)));
        lds_w64(X_CS + w * 256 + (8 * (lane & 7) + 4 * hh + 2 * blk) * 4, v2u{fu(c0), fu(c1)});
      }
      publish(p.ws, WS_HF + jb * 8, tag, tid);
      TS(2);
      if (w == 0) {
        const unsigned a0 = X_CS + lane * 4;
        const float c = ((lds_r32(a0) + lds_r32(a0 + 256)) + (lds_r32(a0 + 512) + lds_r32(a0 + 768))) +
                        ((lds_r32(a0 + 1024) + lds_r32(a0 + 1280)) + (lds_r32(a0 + 1536) + lds_r32(a0 + 1792)));
        if (csum) __builtin_amdgcn_raw_buffer_store_b64(v2u{split2(c), (unsigned)tag}, rs, lane * 32, WS_CS + ((i16 >> 2) * 512 + 64 * (4 * gx + sx)) * 32 + (i16 & 3) * 8, AUX_SC1);
        wait_lgkm0();
      }
    }
    {
      const unsigned tb = (8 * hh + q) * 192 + 32 * blk + 8 * pp + wh * 12288;
      const unsigned ta[3] = {opaque(tb), opaque(tb + STG), opaque(tb + 2 * STG)};
      v4u wr[32][3], hr[32];
      auto rd = [&](int i, int fb) {
        const unsigned a = ta[(i >> 2) % 3] + (i & 3) * 3072 + 64 * fb;
        wr[i][fb] = trfrag(a, a + 768);
      };
      const unsigned hb = WS_H + (dt * 128 + 32 * wh) * 2048 + wl * 1024, hv = lane * 16;
      f32x16 acc[3] = {zero, zero, zero};
      auto rds = [&](int i) { rd(i, 0); rd(i, 1); rd(i, 2); };
      rds(0); issue(1, 0); rds(1); issue(1, 1); rds(2); issue(2, 0); rds(3); issue(2, 1);
      wait_vm<6>(); bar();
      rds(4); rds(5); issue(3, 0); rds(6); rds(7); issue(3, 1);
      wait_vm<6>(); bar();
      rds(8); rds(9); issue(4, 0); rds(10); rds(11); issue(4, 1);
      bar();
      issue(5, 0); issue(5, 1);
      if (wh == 0) {
#pragma unroll
        for (int fb = 0; fb < 3; ++fb)
#pragma unroll
          for (int qd = 0; qd < 4; ++qd) {
            const v2u bv = *(const v2u*)(p.b1 + (f0 + 32 * fb + 8 * qd + 4 * hh) * 2);
            acc[fb][4 * qd] = bflo(bv.x); acc[fb][4 * qd + 1] = bfhi(bv.x); acc[fb][4 * qd + 2] = bflo(bv.y); acc[fb][4 * qd + 3] = bfhi(bv.y);
          }
      }
      PIN(acc[0]); PIN(acc[1]); PIN(acc[2]);
      if (w == 7) {
#pragma unroll 1
        for (unsigned spins = 0; spins < SPIN_CAP; ++spins) {
          const v4u a = ld_sc1(rs, lane * 16, WS_HF + rep);
          const unsigned tl = (unsigned)tag, th = (unsigned)(tag >> 32);
          const bool ok = a.x == tl && a.y == th && a.z == tl && a.w == th;
          if (__all(ok)) break;
          __builtin_amdgcn_s_sleep(1);
          MEMFENCE();
        }
      }
      wait_vm<12>();
      bar();
      TS(3);
      sfor<0, 4>([&](auto I) { constexpr int i = decltype(I)::v; ald_sc1<(i & 1) * 2048>(hr[i], rs, hv, hb + (((i & 30) + 4 * rot) & 31) * 2048); });
      SB();
      sfor<0, 32>([&](auto I) {
        constexpr int i = decltype(I)::v, s = i >> 2, k = i & 3;
        if constexpr (k == 0 && s >= 1 && s <= 5) {
          if constexpr (s == 3) wait_vm<b_wait_g(5, 12)>();
          if constexpr (s == 4) wait_vm<b_wait_g(9, 16)>();
          bar();
          BT(s);
        }
        use_vm<b_wait_h(i)>(hr[i]);
        SB();
        acc[0] = mfma(wr[i][0], hr[i], acc[0]);
        SB();
        if constexpr (b_g(i) == 3) issue(s + 5, k);
        if constexpr (i >= 24) p3_issue(s3, i - 24, w);
        if constexpr (i + 12 < 32) rd(i + 12, 0);
        SB();
        acc[1] = mfma(wr[i][1], hr[i], acc[1]);
        SB();
        if constexpr (b_h(i) >= 1) { constexpr int j = b_hfirst(i); ald_sc1<(j & 1) * 2048>(hr[j], rs, hv, hb + (((j & 30) + 4 * rot) & 31) * 2048); }
        if constexpr (i + 12 < 32) rd(i + 12, 1);
        SB();
        acc[2] = mfma(wr[i][2], hr[i], acc[2]);
        SB();
        if constexpr (b_h(i) == 2) { constexpr int j = b_hfirst(i) + 1; ald_sc1<(j & 1) * 2048>(hr[j], rs, hv, hb + (((j & 30) + 4 * rot) & 31) * 2048); }
        if constexpr (i + 12 < 32) rd(i + 12, 2);
        SB();
      });
      TS(4);
#pragma unroll
      for (int fb = 0; fb < 3; ++fb)
#pragma unroll
        for (int qd = 0; qd < 4; ++qd)
          lds_w128(w * 12288 + (fb * 4 + qd) * 1024 + lane * 16, f4(acc[fb][4 * qd], acc[fb][4 * qd + 1], acc[fb][4 * qd + 2], acc[fb][4 * qd + 3]));
      bar();
      const int nht = w < 4 ? 2 : 1, ht0 = w < 4 ? 2 * w : 4 + w;
#pragma unroll
      for (int e = 0; e < 2; ++e) {
        if (e < nht) {
          const int ht = ht0 + e;
          const unsigned a = (ht & 1) * 12288 + (ht >> 1) * 2048 + lane * 16;
          float s[8];
#pragma unroll
          for (int ww = 0; ww < 4; ++ww) {
            const v4u lo = lds_r128(a + ww * 24576), hi = lds_r128(a + ww * 24576 + 1024);
#pragma unroll
            for (int j = 0; j < 4; ++j) { s[j] = ww ? s[j] + uf(lo[j]) : uf(lo[j]); s[4 + j] = ww ? s[4 + j] + uf(hi[j]) : uf(hi[j]); }
          }
          const v4u uo = {pk(gelu(s[0]), gelu(s[1])), pk(gelu(s[2]), gelu(s[3])), pk(gelu(s[4]), gelu(s[5])), pk(gelu(s[6]), gelu(s[7]))};
          if (ht >= 4 * skip) st_sc1(uo, rs, lane * 16, WS_U + ((dt * 86 + (f0 >> 4) + (ht >> 1)) * 2 + (ht & 1)) * 1024);
        }
      }
      publish(p.ws, WS_UF + (dt * 16 + 4 * sx + ftl) * 8, tag, tid);
      TS(6);
    }
#pragma unroll 1
    for (int i = 8; i < 20; ++i) p3_issue(s3, i, w);
    rd_wo();
    wait_vm<5>();
    bar();
#pragma unroll 1
    for (int i = 20; i < 28; ++i) p3_issue(s3, i, w);
    rd_89();
    bar();
    p3_issue(s3, 28, w); p3_issue(s3, 29, w);
  }

  {
    const int ks0 = 22 * wh;
    const int srow = 64 * st64 + (tid >> 3);
    const v4u xres = *(const v4u*)(p.x + (size_t)srow * 1024 + dt3 * 128 + (tid & 7) * 16);
    const float b2v = bflo(*(const unsigned short*)(p.b2 + srow * 2));
    const unsigned t0[2] = {opaque(s3.b0 + tb3), opaque(s3.b0 + (tb3 ^ 64))};
    const unsigned t1[2] = {opaque(s3.b1 + tb3), opaque(s3.b1 + (tb3 ^ 64))};
    const unsigned t2[2] = {opaque(s3.b2 + tb3), opaque(s3.b2 + (tb3 ^ 64))};
    v4u ar[8][2], ur[22];
    auto rd = [&](int i, int c) {
      const int j = i < 20 ? i : i - 20;
      const unsigned a = j < 8 ? t0[c] + j * ROW : j < 15 ? t1[c] + (j - 8) * ROW : t2[c] + (j - 15) * ROW;
      wr3[i][c] = trfrag(a, a + 1024);
    };
    auto ldu = [&](int k) {
      const int kk = k < s3.kmax ? k : s3.kmax;
      ur[k] = ld_sc1(rs, lane * 16, WS_U + (dt3 * 86 + ks0 + kk) * 2048 + wl * 1024);
    };
    auto ldo = [&](int i) {
      const unsigned so = WS_O + (st64 * 32 + 8 * wh + i) * 2048;
      ar[i][0] = ld_sc1(rs, lane * 16, so); ar[i][1] = ld_sc1(rs, lane * 16 + 1024, so);
    };
    f32x16 acc[2] = {zero, zero};
    TS(7);
    if (w == 7) poll(p.ws, WS_UF + rep + (dt3 * 16 + (lane & 15)) * 8, tag);
    wait_vm<0>();
    bar();
    TS(8);
#pragma unroll
    for (int k = 0; k < 4; ++k) ldu(k);
    SB();
#pragma unroll
    for (int k = 0; k < 22; ++k) {
      const int i = k + 8;
      if (k == OGATE) {
        if (w == 7) poll(p.ws, WS_OF + rep + ((st64 >> 1) * 8 + (lane & 7)) * 8, tag);
        bar();
        TS(9);
      }
      if (k < 20 || wh < 3) {
        acc[0] = mfma(ur[k], wr3[i][0], acc[0]);
        SB();
        if (i + 3 >= 15 && i + 3 < 30) { rd(i + 3, 0); rd(i + 3, 1); }
        if (4 + 2 * k < 22) ldu(4 + 2 * k);
        SB();
        acc[1] = mfma(ur[k], wr3[i][1], acc[1]);
        SB();
        if (5 + 2 * k < 22) ldu(5 + 2 * k);
        if (k >= OGATE && k < OGATE + 8) ldo(k - OGATE);
        SB();
      }
    }
#pragma unroll
    for (int i = 0; i < 8; ++i) {
      acc[0] = mfma(wof[i], ar[i][0], acc[0]);
      acc[1] = mfma(wof[i], ar[i][1], acc[1]);
      SB();
    }
    TS(10);
    bar();
#pragma unroll
    for (int c = 0; c < 2; ++c)
#pragma unroll
      for (int qd = 0; qd < 4; ++qd)
        lds_w128(wh * 17408 + (32 * c + r) * 272 + (32 * wl + 8 * qd + 4 * hh) * 4, f4(acc[c][4 * qd], acc[c][4 * qd + 1], acc[c][4 * qd + 2], acc[c][4 * qd + 3]));
    bar();
    {
      float s[8];
#pragma unroll
      for (int e = 0; e < 4; ++e) { s[2 * e] = bflo(xres[e]) + b2v; s[2 * e + 1] = bfhi(xres[e]) + b2v; }
#pragma unroll
      for (int ww = 0; ww < 4; ++ww) {
        const unsigned a = ww * 17408 + (tid >> 3) * 272 + (tid & 7) * 32;
        const v4u lo = lds_r128(a), hi = lds_r128(a + 16);
#pragma unroll
        for (int j = 0; j < 4; ++j) { s[j] += uf(lo[j]); s[4 + j] += uf(hi[j]); }
      }
      st_sc1(v4u{pk(s[0], s[1]), pk(s[2], s[3]), pk(s[4], s[5]), pk(s[6], s[7])}, __builtin_amdgcn_make_buffer_rsrc(p.out, 0, 2048 * 1024, 0x00020000),
             (tid >> 3) * 1024 + (tid & 7) * 16, st64 * 65536 + dt3 * 128);
    }
    if (b == 0 && tid == 448) *(u64*)p.ws = tag;
  }
}
}

extern "C" void kernel_launch(void* const* d_in, const int* in_sizes, int n_in, void* d_out, int out_size, void* d_ws,
                              size_t ws_size, hipStream_t stream) {
  hk::Params p;
  p.x = (const char*)d_in[0]; p.wqkv = (const char*)d_in[1]; p.wphi = (const char*)d_in[2]; p.wo = (const char*)d_in[3];
  p.w1 = (const char*)d_in[5]; p.b1 = (const char*)d_in[6]; p.w2 = (const char*)d_in[7]; p.b2 = (const char*)d_in[8];
  p.out = (char*)d_out; p.ws = (char*)d_ws;
  (void)hipFuncSetAttribute((const void*)hk::fused, hipFuncAttributeMaxDynamicSharedMemorySize, hk::LDS_BYTES);
  hk::fused<<<256, 512, hk::LDS_BYTES, stream>>>(p);
}
```

```cpp
#include <hip/hip_runtime.h>
#include <stdint.h>

#define GAS __attribute__((address_space(1)))
#define LAS __attribute__((address_space(3)))
#define DI __device__ __forceinline__
typedef short bf16x8 __attribute__((ext_vector_type(8)));
typedef short s16x4 __attribute__((ext_vector_type(4)));
typedef __bf16 bf2 __attribute__((ext_vector_type(2)));
typedef float f32x16 __attribute__((ext_vector_type(16)));
typedef float f32x2 __attribute__((ext_vector_type(2)));
typedef unsigned v4u __attribute__((ext_vector_type(4)));
typedef unsigned v2u __attribute__((ext_vector_type(2)));
typedef unsigned long long u64;
typedef GAS u64 gu64;

namespace hk {
constexpr unsigned FS = 128, REPB = 5 * 128 * FS;
constexpr unsigned WS_HF = 25u << 19, WS_KF = WS_HF + 128 * FS, WS_OF = WS_KF + 128 * FS, WS_UF = WS_OF + 128 * FS, WS_PF = WS_UF + 128 * FS;
constexpr unsigned WS_ST = 98304, WS_ZP = 131072, WS_KVP = 196608, WS_CS = 720896;
constexpr unsigned WS_H = 1u << 20, WS_U = 3u << 20, WS_O = 9u << 19, WS_PB = 13u << 19, WS_BYTES = 25u << 19;
constexpr unsigned REG = 40960, ROW = 8192, STG = 49152, LDS_BYTES = 163840;
constexpr unsigned CODE_BYTES = 36096;
constexpr unsigned SPIN_CAP = 1u << 14;
constexpr int AUX_SC1 = 16;
constexpr float LOG2E = 1.4426950408889634f;

struct Params {
  const char *x, *wqkv, *wphi, *wo, *w1, *b1, *w2, *b2;
  char *out, *ws;
};

DI f32x16 mfma(v4u a, v4u b, f32x16 c) {
  return __builtin_amdgcn_mfma_f32_32x32x16_bf16(__builtin_bit_cast(bf16x8, a), __builtin_bit_cast(bf16x8, b), c, 0, 0, 0);
}
DI unsigned pk(float a, float b) { bf2 v = {(__bf16)a, (__bf16)b}; return __builtin_bit_cast(unsigned, v); }
DI unsigned pk2(f32x2 a) { return pk(a.x, a.y); }
DI float bflo(unsigned u) { return __uint_as_float(u << 16); }
DI float bfhi(unsigned u) { return __uint_as_float(u & 0xffff0000u); }
DI float uf(unsigned u) { return __uint_as_float(u); }
DI unsigned fu(float f) { return __float_as_uint(f); }
template <int O> DI v4u pack8(const f32x16& x) {
  return v4u{pk(x[O], x[O + 1]), pk(x[O + 2], x[O + 3]), pk(x[O + 4], x[O + 5]), pk(x[O + 6], x[O + 7])};
}
DI v4u f4(float a, float b, float c, float d) { return v4u{fu(a), fu(b), fu(c), fu(d)}; }
DI v2u trr(unsigned a) { return __builtin_bit_cast(v2u, __builtin_amdgcn_ds_read_tr16_b64_v4i16((LAS s16x4*)(size_t)a)); }
DI v4u trfrag(unsigned a0, unsigned a1) { v2u lo = trr(a0), hi = trr(a1); return v4u{lo.x, lo.y, hi.x, hi.y}; }
DI v4u lds_r128(unsigned a) { return *(LAS v4u*)(size_t)a; }
DI v2u lds_r64(unsigned a) { return *(LAS v2u*)(size_t)a; }
DI float lds_r32(unsigned a) { return *(LAS float*)(size_t)a; }
DI void lds_w128(unsigned a, v4u v) { *(LAS v4u*)(size_t)a = v; }
DI void lds_w64(unsigned a, v2u v) { *(LAS v2u*)(size_t)a = v; }
DI void lds_w32(unsigned a, float v) { *(LAS float*)(size_t)a = v; }
DI void glds1(unsigned lds, const char* sb, unsigned v0) {
  asm volatile("s_mov_b32 m0, %0\n\ts_nop 0\n\tglobal_load_lds_dwordx4 %2, %1" ::"s"(lds), "s"(sb), "v"(v0) : "memory");
}
DI void glds2(unsigned lds, const char* sb, unsigned v0, unsigned v1) {
  asm volatile("s_mov_b32 m0, %0\n\ts_nop 0\n\tglobal_load_lds_dwordx4 %2, %1\n\tglobal_load_lds_dwordx4 %3, %1 offset:1024"
               ::"s"(lds), "s"(sb), "v"(v0), "v"(v1) : "memory");
}
DI void glds3(unsigned lds, const char* sb, unsigned v0, unsigned v1, unsigned v2) {
  asm volatile("s_mov_b32 m0, %0\n\ts_nop 0\n\tglobal_load_lds_dwordx4 %2, %1\n\tglobal_load_lds_dwordx4 %3, %1 offset:1024\n\t"
               "global_load_lds_dwordx4 %4, %1 offset:2048"
               ::"s"(lds), "s"(sb), "v"(v0), "v"(v1), "v"(v2) : "memory");
}
#define SB() __builtin_amdgcn_sched_barrier(0)
#define PIN(x) asm volatile("" : "+v"(x))
#define MEMFENCE() asm volatile("" ::: "memory")
DI unsigned opaque(unsigned v) { asm volatile("" : "+v"(v)); return v & 0x3ffffu; }
DI unsigned rfl(unsigned v) { return __builtin_amdgcn_readfirstlane(v); }
template <int N> DI void wait_vm() { asm volatile("s_waitcnt vmcnt(%0)" ::"n"(N) : "memory"); }
DI void wait_lgkm0() { asm volatile("s_waitcnt lgkmcnt(0)" ::: "memory"); }
DI void bar() { asm volatile("s_waitcnt lgkmcnt(0)" ::: "memory"); __builtin_amdgcn_s_barrier(); MEMFENCE(); }
DI v4u ld_sc1(__amdgpu_buffer_rsrc_t rs, unsigned voff, unsigned soff) { return __builtin_amdgcn_raw_buffer_load_b128(rs, voff, soff, AUX_SC1); }
DI void st_sc1(v4u v, __amdgpu_buffer_rsrc_t rs, unsigned voff, unsigned soff) {
  __builtin_amdgcn_raw_buffer_store_b128(v, rs, voff, soff, AUX_SC1);
  asm volatile("s_nop 1" ::"v"(v) : "memory");
}
template <int OFF> DI void ald_sc1(v4u& d, __amdgpu_buffer_rsrc_t rs, unsigned voff, unsigned soff) {
  asm volatile("buffer_load_dwordx4 %0, %1, %2, %3 offen offset:%4 sc1" : "=v"(d) : "v"(voff), "s"(rs), "s"(soff), "n"(OFF) : "memory");
}
DI void touch(unsigned& d, const char* sb, unsigned vo) { asm volatile("global_load_dword %0, %1, %2" : "+v"(d) : "v"(vo), "s"(sb) : "memory"); }
template <int N> DI void use_vm(v4u& d) { asm volatile("s_waitcnt vmcnt(%1)" : "+v"(d) : "n"(N) : "memory"); }

DI void publish(char* ws, unsigned off, u64 tag, int tid, bool stored = true) {
  unsigned vo = (tid & 7) * REPB + off;
  v2u tv = {(unsigned)tag, (unsigned)(tag >> 32)};
  PIN(vo); PIN(tv);
  if (stored) wait_vm<0>();
  bar();
  if (tid < 8) __hip_atomic_store((gu64*)(ws + vo), __builtin_bit_cast(u64, tv), __ATOMIC_RELAXED, __HIP_MEMORY_SCOPE_AGENT);
  MEMFENCE();
}
DI void poll(char* ws, unsigned off, u64 tag) {
  gu64* p = (gu64*)(ws + off);
  __builtin_amdgcn_s_setprio(3);
#pragma unroll 1
  for (unsigned spins = 0; spins < SPIN_CAP; ++spins) {
    u64 v = __hip_atomic_load(p, __ATOMIC_RELAXED, __HIP_MEMORY_SCOPE_AGENT);
    if (__all(v == tag)) break;
    __builtin_amdgcn_s_sleep(1);
  }
  __builtin_amdgcn_s_setprio(0);
  __builtin_amdgcn_fence(__ATOMIC_ACQUIRE, "wavefront");
  MEMFENCE();
}

template <int CTRL> DI float dpp_add(float v) { return v + uf(__builtin_amdgcn_update_dpp(0u, fu(v), CTRL, 0xf, 0xf, true)); }
DI float row_sum(float v) { return dpp_add<0x140>(dpp_add<0x141>(dpp_add<0x4E>(dpp_add<0xB1>(v)))); }
DI float sw16_add(float a, float b) { auto r = __builtin_amdgcn_permlane16_swap(fu(a), fu(b), false, false); return uf(r[0]) + uf(r[1]); }
DI float sw32_add(float a, float b) { auto r = __builtin_amdgcn_permlane32_swap(fu(a), fu(b), false, false); return uf(r[0]) + uf(r[1]); }
DI void sw32_split(float m, float& a, float& b) { auto q = __builtin_amdgcn_permlane32_swap(fu(m), fu(m), false, false); a = uf(q[0]); b = uf(q[1]); }
DI float mx(float a, float b) { return __builtin_elementwise_maximum(a, b); }
DI float mn(float a, float b) { return __builtin_elementwise_minimum(a, b); }
template <int O> DI void hh_exp(const f32x16& P, float c, f32x2 (&ep)[4], f32x2 (&en)[4]) {
  const float hi = mx(mx(mx(P[O], P[O + 1]), P[O + 2]), mx(mx(mx(P[O + 3], P[O + 4]), P[O + 5]), mx(P[O + 6], P[O + 7])));
  const float lo = mn(mn(mn(P[O], P[O + 1]), P[O + 2]), mn(mn(mn(P[O + 3], P[O + 4]), P[O + 5]), mn(P[O + 6], P[O + 7])));
  auto r = __builtin_amdgcn_permlane32_swap(fu(hi), fu(-lo), false, false);
  float bp, bn;
  sw32_split(mx(uf(r[0]), uf(r[1])) * -c, bp, bn);
  const f32x2 c2 = {c, c}, bp2 = {bp, bp}, bn2 = {bn, bn};
#pragma unroll
  for (int j = 0; j < 4; ++j) {
    const f32x2 pv = {P[O + 2 * j], P[O + 2 * j + 1]};
    const f32x2 a = __builtin_elementwise_fma(pv, c2, bp2), b = __builtin_elementwise_fma(pv, -c2, bn2);
    ep[j] = f32x2{__builtin_amdgcn_exp2f(a.x), __builtin_amdgcn_exp2f(a.y)};
    en[j] = f32x2{__builtin_amdgcn_exp2f(b.x), __builtin_amdgcn_exp2f(b.y)};
  }
}
template <int O> DI void hedgehog(const f32x16& P, float c, v4u& fp, v4u& fn) {
  f32x2 ep[4], en[4];
  hh_exp<O>(P, c, ep, en);
  const f32x2 s2 = (ep[0] + ep[1]) + (ep[2] + ep[3]), t2 = (en[0] + en[1]) + (en[2] + en[3]);
  float rp, rn;
  sw32_split(__builtin_amdgcn_rcpf(sw32_add(s2.x + s2.y, t2.x + t2.y)), rp, rn);
  const f32x2 rp2 = {rp, rp}, rn2 = {rn, rn};
  fp = v4u{pk2(ep[0] * rp2), pk2(ep[1] * rp2), pk2(ep[2] * rp2), pk2(ep[3] * rp2)};
  fn = v4u{pk2(en[0] * rn2), pk2(en[1] * rn2), pk2(en[2] * rn2), pk2(en[3] * rn2)};
}
DI unsigned split2(float v) {
  const unsigned a = pk(v, 0.f);
  return pk(v, v - bflo(a));
}
DI float gelu(float x) {
  const float e = x * __builtin_fmaf(x * x, -2.f * LOG2E * 0.044715f * 0.7978845608028654f, -2.f * LOG2E * 0.7978845608028654f);
  return x * __builtin_amdgcn_rcpf(1.f + __builtin_amdgcn_exp2f(e));
}
DI u64 mix64(u64 z) {
  z += 0x9E3779B97F4A7C15ull; z = (z ^ (z >> 30)) * 0xBF58476D1CE4E5B9ull; z = (z ^ (z >> 27)) * 0x94D049BB133111EBull;
  return z ^ (z >> 31);
}

constexpr int DH = 10;
constexpr int OGATE = 12;
constexpr int b_g(int i) { return (i == 0 || i == 2 || i == 8 || i == 10) ? 3 : i >= 24 ? 1 : 0; }
constexpr int b_h(int i) { return i < DH - 4 ? 2 : i + DH < 32 ? 1 : 0; }
constexpr int b_hfirst(int i) { return i < DH - 4 ? 4 + 2 * i : i + DH; }
constexpr int b_wait_h(int i, int at) {
  int n = 0; bool seen = false;
  for (int j = 0; j < 4; ++j) { if (seen) ++n; if (j == i) seen = true; }
  for (int s = 0; s < at; ++s) {
    if (seen) n += b_g(s);
    for (int j = 0; j < b_h(s); ++j) { if (seen) ++n; if (b_hfirst(s) + j == i) seen = true; }
  }
  return n;
}
constexpr int b_wait_g(int from, int at) {
  int n = from >= 0 ? b_h(from) : 0;
  for (int s = from + 1; s < at; ++s) n += b_g(s) + b_h(s);
  return n;
}
template <int I> struct IC { static constexpr int v = I; };
template <int LO, int HI, class F> DI void sfor(F&& f) { if constexpr (LO < HI) { f(IC<LO>{}); sfor<LO + 1, HI>(f); } }

struct P3Src { const char* wo; const char* w2; unsigned vo, v2; int kmax; unsigned b0, b1, b2; };
DI void p3_issue(const P3Src& s, int i, int w) {
  const int j = i < 20 ? i : i - 20;
  const unsigned lds = rfl((j < 8 ? s.b0 + j * ROW : j < 15 ? s.b1 + (j - 8) * ROW : s.b2 + (j - 15) * ROW) + w * 1024);
  if (i < 8) glds1(lds, s.wo + i * 16 * 1024, s.vo);
  else { int k = i - 8; k = k < s.kmax ? k : s.kmax; glds1(lds, s.w2 + (size_t)k * 65536, s.v2); }
}

__global__ __launch_bounds__(512, 2) void fused(Params p) {
  extern __shared__ __attribute__((aligned(16))) char smem[];
  int tid = threadIdx.x, b = blockIdx.x;
  const int w = rfl(tid >> 6), wh = w >> 1, wl = w & 1;
  v2u epoch;
  {
    const unsigned l = tid & 63, xg = b & 7, i16 = (b >> 3) & 15, sx = xg >> 1, gx = xg & 1;
    if (w == 7) {
      const char* pc;
      asm volatile("s_getpc_b64 %0" : "=s"(pc));
      const unsigned o = (i16 & 7) * 8192 + l * 128;
      asm volatile("s_mov_b32 m0, %0\n\ts_nop 0\n\tglobal_load_lds_dword %2, %1" ::"s"(3 * REG), "s"(pc), "v"(o < CODE_BYTES ? o : 0u) : "memory");
    }
    asm volatile("global_load_dwordx2 %0, %1, %2 sc1" : "=v"(epoch) : "v"(0u), "s"(p.ws) : "memory");
    const unsigned r8 = l >> 3, c8 = l & 7;
    if (b < 128) {
      const unsigned xc = (c8 ^ (l >> 4)) * 16, vw0 = r8 * 3072 + (c8 ^ ((l >> 2) & 4)) * 16;
      const char* xs = p.x + (size_t)((4 * sx + (i16 & 3)) * 131072 + w * 16384);
      const char* ws_ = p.wqkv + (size_t)(w * 24576 + (4 * gx + (i16 >> 2)) * 128);
#pragma unroll
      for (int c = 0; c < 2; ++c) {
        glds2(c * REG + w * 2048, xs + c * 128, r8 * 1024 + xc, r8 * 1024 + (xc ^ 64) + 8192 - 1024);
#pragma unroll
        for (int t = 0; t < 3; ++t) glds1(c * REG + 16384 + t * 8192 + w * 1024, ws_ + c * 196608 + t * 1024, vw0);
      }
    } else {
      glds2(18 * ROW + w * 2048, p.x + (size_t)((16 * xg + i16) * 16384 + w * 2048), l * 16, l * 16);
      glds2(STG + w * 2048, p.wqkv + (size_t)((128 * (i16 & 3) + 16 * w) * 3072 + (i16 < 12 ? (i16 >> 2) * 1024 : 0) + (4 * gx + sx) * 128),
            r8 * 3072 + c8 * 16, r8 * 3072 + c8 * 16 + 24576 - 1024);
    }
  }
  asm volatile("" : "+s"(b));
  int lane, r, hh, blk, q, pp;
  unsigned tb3;
  auto relane = [&]() {
    PIN(tid);
    lane = tid & 63; r = lane & 31; hh = lane >> 5; blk = (lane >> 4) & 1; q = (lane >> 2) & 3; pp = lane & 3;
    tb3 = wh * 2048 + (4 * hh + q) * 128 + ((32 * blk + 8 * pp) ^ ((q >> 1) << 6));
  };
  relane();
  const int xg = b & 7, idx = b >> 3, sx = xg >> 1, gx = xg & 1, i16 = idx & 15;
  const bool roleA = idx < 16;
  u64 tag;
  auto make_tag = [&]() {
    unsigned lo, hi;
    asm volatile("v_readfirstlane_b32 %0, %2\n\tv_readfirstlane_b32 %1, %3" : "=s"(lo), "=s"(hi) : "v"(epoch.x), "v"(epoch.y) : "memory");
    tag = mix64(((u64)hi << 32) | lo);
    asm volatile("" : "+s"(tag));
  };
  const __amdgpu_buffer_rsrc_t rs = __builtin_amdgcn_make_buffer_rsrc(p.ws, 0, WS_BYTES, 0x00020000);
  const unsigned rep = xg * REPB;
  asm volatile("" ::"v"((LAS char*)smem));
  f32x16 zero;
#pragma unroll
  for (int e = 0; e < 16; ++e) zero[e] = 0.f;

  const int st64 = 8 * sx + (i16 & 7);
  const int dt3 = 4 * gx + (((i16 >> 3) + 2 * (i16 & 1) + 2 * (idx >> 4)) & 3);
  const unsigned ch4 = ((lane & 7) ^ (((lane >> 4) & 1) << 2)) * 16;
  P3Src s3;
  auto p3_lanes = [&]() {
    const unsigned row = 8 * wl + (lane >> 3), c4 = ((lane & 7) ^ (((lane >> 4) & 1) << 2)) * 16;
    s3.vo = row * 1024 + c4; s3.v2 = row * 4096 + c4;
  };
  {
    s3.wo = p.wo + (size_t)(128 * wh) * 1024 + dt3 * 128;
    s3.w2 = p.w2 + (size_t)(22 * wh) * 65536 + st64 * 128;
    s3.kmax = wh < 3 ? 21 : 19;
    s3.b0 = roleA ? 0 : 12 * ROW; s3.b1 = roleA ? 8 * ROW : 0; s3.b2 = roleA ? 10 * ROW : 7 * ROW;
  }
  v4u wof[8], wr3[30][2];
  auto rd_wo = [&](int lo, int hi) {
#pragma unroll
    for (int i = lo; i < hi; ++i) { const unsigned a = s3.b0 + (tb3 ^ (wl << 6)) + i * ROW; wof[i] = trfrag(a, a + 1024); }
  };
  auto rd_w2 = [&](int lo, int hi) {
#pragma unroll
    for (int i = lo; i < hi; ++i)
#pragma unroll
      for (int c = 0; c < 2; ++c) { const unsigned a = s3.b1 + (tb3 ^ (c << 6)) + (i - 8) * ROW; wr3[i][c] = trfrag(a, a + 1024); }
  };

  if (roleA) {
    p3_lanes();
    const int st = 4 * sx + (idx & 3), g = 4 * gx + (idx >> 2), rg = wh, cg = wl, pair = 2 * g + cg;
    const unsigned xc = ((lane & 7) ^ (lane >> 4)) * 16, xr = (lane >> 3) * 1024;
    const unsigned vx0 = xr + xc, vx1 = xr + (xc ^ 64) + 8192;
    const unsigned vw0 = (lane >> 3) * 3072 + ch4;
    const char* xs = p.x + (size_t)(128 * st + 16 * w) * 1024;
    const char* wsrc = p.wqkv + (size_t)(8 * w) * 3072 + g * 128;
    auto issue = [&](int c, int n) {
      const unsigned sl = (c & 3) * REG;
      if (n < 2) glds1(sl + w * 2048 + n * 1024, xs + c * 128, n ? vx1 : vx0);
      else glds1(sl + 16384 + (n - 2) * 8192 + w * 1024, wsrc + (size_t)c * 64 * 3072 + (n - 2) * 1024, vw0);
    };
    unsigned tcha = 0;
    v4u wph;
    asm volatile("global_load_dwordx4 %0, %1, %2" : "=v"(wph) : "v"(lane * 16), "s"(p.wphi + pair * 1024) : "memory");
    v4u id0, id1;
    {
      const v4u z4 = {0, 0, 0, 0};
      const int rr = (r & 15) - 4 * hh;
      v4u e1;
      e1.x = rr == 0 ? 0x3f80u : rr == 1 ? 0x3f800000u : 0u; e1.y = rr == 2 ? 0x3f80u : rr == 3 ? 0x3f800000u : 0u;
      e1.z = rr == 8 ? 0x3f80u : rr == 9 ? 0x3f800000u : 0u; e1.w = rr == 10 ? 0x3f80u : rr == 11 ? 0x3f800000u : 0u;
      id0 = r < 16 ? e1 : z4; id1 = r < 16 ? z4 : e1;
      PIN(id0); PIN(id1);
    }
    f32x16 acc[3] = {zero, zero, zero};
    unsigned xa[2][4], wa[4];
#pragma unroll
    for (int k = 0; k < 4; ++k) {
      xa[0][k] = opaque((32 * rg + r) * 128 + (unsigned)(((2 * k + hh) ^ ((r >> 1) & 7)) << 4)); xa[1][k] = opaque(xa[0][k] + 2 * REG);
      wa[k] = opaque(k * REG + 16384 + (8 * hh + q) * 128 + ((64 * cg + 32 * blk + 8 * pp) ^ ((q >> 1) << 6)));
    }
    v4u fx[8][4], fw[8][4][3], stv, cg3[3];
    auto rdk = [&](int c, int k, int t) {
      if (t == 0) fx[c][k] = lds_r128(xa[(c >> 1) & 1][k] + (c & 1) * REG);
      const unsigned a = wa[c & 3] + k * 2048 + t * 8192;
      fw[c][k][t] = trfrag(a, a + 512);
    };
    auto ld_stat = [&](int t) {
      if (t == 3) ald_sc1<0>(stv, rs, r * 16, WS_ST + (128 * st + 32 * rg) * 16);
      else ald_sc1<0>(cg3[t], rs, r * 32 + hh * 16, WS_CS + (512 * t + 32 * pair) * 32);
    };
    auto feed = [&](int c) {
#pragma unroll
      for (int n = 0; n < 5; ++n) { if (c < 5) issue(c + 3, n); }
    };
    PIN(acc[0]); PIN(acc[1]); PIN(acc[2]);
    wait_vm<6>();
    bar();
#pragma unroll
    for (int k = 0; k < 4; ++k) { rdk(0, k, 0); rdk(0, k, 1); rdk(0, k, 2); }
    feed(-1);
    make_tag();
#pragma unroll
    for (int c = 0; c < 8; ++c) {
      if (c < 7) wait_vm<5>();
      bar();
      if (c == 5) { ld_stat(0); ld_stat(1); ld_stat(2); ld_stat(3); }
      if (c == 7) { PIN(wph); lds_w128(15 * ROW + w * 4096 + lane * 16, wph); }
      if (w >= 4) feed(c);
#pragma unroll
      for (int k = 0; k < 4; ++k)
#pragma unroll
        for (int t = 0; t < 3; ++t) {
          SB();
          acc[t] = mfma(fw[c][k][t], fx[c][k], acc[t]);
          SB();
          if (c < 7) rdk(c + 1, k, t);
          if (c == 7 && k == 1 && t == 0) { const unsigned a = 15 * ROW + w * 4096 + blk * 512 + (4 * hh + q) * 32 + 8 * pp; wph = trfrag(a, a + 256); }
        }
      SB();
      if (w < 4) feed(c);
      if (c == 5) { const unsigned r0 = 11 * (i16 * 8 + w) + (lane >> 3); touch(tcha, p.w2 + sx * 1024, (r0 < 1376 ? r0 : 1375) * 4096 + (lane & 7) * 128); }
      if (c == 6) {
        const unsigned u = i16 * 8 + w, r1 = 11 * u + 8 + (lane >> 3);
        const char* a1 = lane < 24 ? p.w2 + sx * 1024 + (size_t)(r1 < 1376 ? r1 : 1375) * 4096 + (lane & 7) * 128
                                   : p.wo + gx * 512 + (4 * u + ((lane >> 2) & 3)) * 1024 + (lane & 3) * 128;
        asm volatile("global_load_dword %0, %1, off" : "+v"(tcha) : "v"(a1) : "memory");
      }
    }
    {
      const unsigned tl = (unsigned)tag;
      asm volatile("s_waitcnt vmcnt(2)" : "+v"(stv), "+v"(cg3[0]), "+v"(cg3[1]), "+v"(cg3[2])::"memory");
#pragma unroll 1
      for (unsigned spins = 0; spins < SPIN_CAP; ++spins) {
        const bool ok = stv.y == tl && stv.w == tl && cg3[0].y == tl && cg3[0].w == tl && cg3[1].y == tl && cg3[1].w == tl && cg3[2].y == tl && cg3[2].w == tl;
        if (__all(ok)) break;
        ld_stat(0); ld_stat(1); ld_stat(2); ld_stat(3);
        asm volatile("s_waitcnt vmcnt(0)" : "+v"(stv), "+v"(cg3[0]), "+v"(cg3[1]), "+v"(cg3[2])::"memory");
      }
    }
    v4u aphi0, aphi1;
    {
      const v4u z4 = {0, 0, 0, 0};
      aphi0 = r < 16 ? wph : z4; aphi1 = r < 16 ? z4 : wph;
    }
    const unsigned EX = 15 * ROW, X_Z = 19 * ROW;
    const float rstd = uf(stv.z);
    {
      const v4u mf = {stv.x, stv.x, stv.x, stv.x};
#pragma unroll
      for (int t = 1; t < 4; ++t) {
        const v4u c = cg3[t % 3];
        const v4u cf = {__builtin_amdgcn_perm(c.x, c.x, 0x01000100u), __builtin_amdgcn_perm(c.x, c.x, 0x03020302u),
                        __builtin_amdgcn_perm(c.z, c.z, 0x01000100u), __builtin_amdgcn_perm(c.z, c.z, 0x03020302u)};
        acc[t % 3] = mfma(cf, mf, acc[t % 3]);
      }
    }
    const float cexp = rstd * LOG2E;
    {
      f32x16 P = mfma(aphi0, pack8<0>(acc[1]), zero);
      P = mfma(aphi1, pack8<8>(acc[1]), P);
      f32x16 vs;
#pragma unroll
      for (int e = 0; e < 16; ++e) vs[e] = acc[2][e] * rstd;
      f32x16 zv = mfma(pack8<0>(vs), id0, zero);
      zv = mfma(pack8<8>(vs), id1, zv);
      v4u fp, fn, gp, gn;
      hedgehog<0>(P, cexp, fp, fn);
      hedgehog<8>(P, cexp, gp, gn);
      f32x16 zf = mfma(fp, id0, zero), zg = mfma(gp, id0, zero);
      zf = mfma(fn, id1, zf); zg = mfma(gn, id1, zg);
      const v4u vt0 = pack8<0>(zv), vt1 = pack8<8>(zv);
      f32x16 kv0 = mfma(pack8<0>(zf), vt0, zero), kv1 = mfma(pack8<0>(zg), vt0, zero);
      kv0 = mfma(pack8<8>(zf), vt1, kv0); kv1 = mfma(pack8<8>(zg), vt1, kv1);
      f32x2 z0 = {zf[0], zf[1]}, z1 = {zg[0], zg[1]};
#pragma unroll
      for (int e = 2; e < 16; e += 2) { z0 += f32x2{zf[e], zf[e + 1]}; z1 += f32x2{zg[e], zg[e + 1]}; }
      lds_w32(X_Z + w * 256 + lane * 4, sw32_add(z0.x + z0.y, z1.x + z1.y));
#pragma unroll
      for (int e = 0; e < 16; ++e) kv0[e] = r < 16 ? kv0[e] : kv1[e];
#pragma unroll
      for (int i = 0; i < 4; ++i) lds_w128(EX + w * 4096 + i * 1024 + lane * 16, f4(kv0[4 * i], kv0[4 * i + 1], kv0[4 * i + 2], kv0[4 * i + 3]));
      wait_vm<0>();
      bar();
      PIN(tcha);
      if (rg < 2) {
        float s[8];
#pragma unroll
        for (int i = 0; i < 4; ++i) {
          const unsigned a = EX + (2 * i + cg) * 4096 + rg * 2048 + lane * 16;
          const v4u lo = lds_r128(a), hi = lds_r128(a + 1024);
#pragma unroll
          for (int e = 0; e < 4; ++e) { s[e] = i ? s[e] + uf(lo[e]) : uf(lo[e]); s[4 + e] = i ? s[4 + e] + uf(hi[e]) : uf(hi[e]); }
        }
        st_sc1(v4u{pk(s[0], s[1]), pk(s[2], s[3]), pk(s[4], s[5]), pk(s[6], s[7])}, rs, lane * 16, WS_KVP + ((pair * 16 + st) * 2 + rg) * 1024);
      } else if (rg == 2) {
        const unsigned a = X_Z + cg * 256 + lane * 4;
        const float zt = (lds_r32(a) + lds_r32(a + 512)) + (lds_r32(a + 1024) + lds_r32(a + 1536));
        __builtin_amdgcn_raw_buffer_store_b32(fu(zt), rs, lane * 4, WS_ZP + (pair * 16 + st) * 256, AUX_SC1);
      }
    }
    publish(p.ws, WS_KF + (g * 16 + st) * FS, tag, tid);
    if (w >= 4) {
#pragma unroll
      for (int i = 0; i < 15; ++i) p3_issue(s3, i, w);
    }
    v4u fq[2][2];
    {
      f32x16 P = mfma(aphi0, pack8<0>(acc[0]), zero);
      P = mfma(aphi1, pack8<8>(acc[0]), P);
      hedgehog<0>(P, cexp, fq[0][0], fq[0][1]);
      hedgehog<8>(P, cexp, fq[1][0], fq[1][1]);
      PIN(fq[0][0]); PIN(fq[0][1]); PIN(fq[1][0]); PIN(fq[1][1]);
    }
    if (w < 4) {
#pragma unroll
      for (int i = 0; i < 15; ++i) p3_issue(s3, i, w);
    }
    if (w == 7) poll(p.ws, WS_KF + rep + (g * 16 + (lane & 15)) * FS, tag);
    wait_vm<0>();
    bar();
    {
      v4u kp[8];
#pragma unroll
      for (int i = 0; i < 8; ++i) kp[i] = ld_sc1(rs, lane * 16, WS_KVP + (pair * 32 + (rg & 1) + 2 * (8 * (rg >> 1) + i)) * 1024);
      if (rg == 0) {
        v4u zp[4];
#pragma unroll
        for (int i = 0; i < 4; ++i) zp[i] = ld_sc1(rs, lane * 16, WS_ZP + pair * 4096 + i * 1024);
        float zs[4];
#pragma unroll
        for (int e = 0; e < 4; ++e) {
          float t = (uf(zp[0][e]) + uf(zp[1][e])) + (uf(zp[2][e]) + uf(zp[3][e]));
          t = sw16_add(t, t); zs[e] = sw32_add(t, t);
        }
        lds_w64(X_Z + w * 256 + (lane & 15) * 8, v2u{pk(zs[0], zs[1]), pk(zs[2], zs[3])});
      }
      MEMFENCE();
      rd_wo(0, 8); rd_w2(8, 15);
      f32x16 Ssum = zero;
#pragma unroll
      for (int i = 0; i < 8; ++i) Ssum = mfma(id0, kp[i], Ssum);
      lds_w128(EX + w * 4096 + lane * 16, f4(Ssum[0], Ssum[1], Ssum[2], Ssum[3]));
      lds_w128(EX + w * 4096 + 1024 + lane * 16, f4(Ssum[4], Ssum[5], Ssum[6], Ssum[7]));
      bar();
      if (w >= 4) {
#pragma unroll
        for (int i = 15; i < 30; ++i) p3_issue(s3, i, w);
      }
      v4u ah[2][2];
#pragma unroll
      for (int t = 0; t < 2; ++t) {
        const unsigned a = EX + (2 * t + cg) * 4096 + lane * 16;
        const v4u a0 = lds_r128(a), a1 = lds_r128(a + 1024), b0 = lds_r128(a + 16384), b1 = lds_r128(a + 16384 + 1024);
        float s[8];
#pragma unroll
        for (int e = 0; e < 4; ++e) { s[e] = uf(a0[e]) + uf(b0[e]); s[4 + e] = uf(a1[e]) + uf(b1[e]); }
        const v4u m = {pk(s[0], s[1]), pk(s[2], s[3]), pk(s[4], s[5]), pk(s[6], s[7])};
#pragma unroll
        for (int h = 0; h < 2; ++h) {
          const unsigned za = X_Z + cg * 256 + (32 * h + 16 * t + 4 * hh) * 2;
          const v2u lo = lds_r64(za), hi = lds_r64(za + 16);
          const v4u zf = {lo.x, lo.y, hi.x, hi.y};
          ah[h][t] = ((r < 16) == (h == 0)) ? m : zf;
        }
      }
      const unsigned ob = WS_O + (((2 * st + (rg >> 1)) * 32 + 2 * pair) * 2 + (rg & 1)) * 1024;
      f32x16 N0 = mfma(ah[0][0], fq[0][0], zero), N1 = mfma(ah[1][0], fq[1][0], zero);
      N0 = mfma(ah[0][1], fq[0][1], N0); N1 = mfma(ah[1][1], fq[1][1], N1);
      const float r0 = __builtin_amdgcn_rcpf(N0[8] + 1e-6f), r1 = __builtin_amdgcn_rcpf(N1[0] + 1e-6f);
#pragma unroll
      for (int e = 0; e < 8; ++e) { N0[e] *= r0; N1[8 + e] *= r1; }
      st_sc1(pack8<0>(N0), rs, lane * 16, ob);
      st_sc1(pack8<8>(N1), rs, lane * 16, ob + 2048);
    }
    publish(p.ws, WS_OF + (st * 8 + g) * FS, tag, tid);
    if (w < 4) {
#pragma unroll
      for (int i = 15; i < 30; ++i) p3_issue(s3, i, w);
    }
  } else {
    const int jb = 16 * xg + i16;
    const int dT = i16 & 3, fT = 4 * gx + (i16 >> 2);
    const int f0 = fT < 7 ? 192 * fT : 1184;
    unsigned vw[3];
#pragma unroll
    for (int n = 0; n < 3; ++n) { const unsigned id = 64 * n + lane, row = id / 24, ch = (id % 24) ^ ((row & 2) << 1); vw[n] = row * 2752 + ch * 16 - 1024 * n; }
    const int rot = (dT + fT) & 3;
    const char* w1s = p.w1 + f0 * 2 + (size_t)(512 * sx + 16 * w) * 2752;
    auto issue = [&](int s, int e) {
      glds3((s == 1 ? 2 : s == 2 ? 1 : 0) * STG + w * 6144 + e * 3072, w1s + (128 * ((s + rot) & 3) + 8 * e) * 2752, vw[0], vw[1], vw[2]);
    };
    {
      const bool csum = i16 < 12;
      if (w >= 4) { issue(0, -8); issue(0, -7); }
      if (w >= 4) wait_vm<6>(); else wait_vm<0>();
      make_tag();
      const v4u xr[2] = {lds_r128(18 * ROW + w * 2048 + lane * 16), lds_r128(18 * ROW + w * 2048 + 1024 + lane * 16)};
      f32x2 v[2][4];
      float a2[2];
#pragma unroll
      for (int i = 0; i < 2; ++i) {
#pragma unroll
        for (int e = 0; e < 4; ++e) v[i][e] = f32x2{bflo(xr[i][e]), bfhi(xr[i][e])};
        const f32x2 s = (v[i][0] + v[i][1]) + (v[i][2] + v[i][3]);
        const f32x2 sq = __builtin_elementwise_fma(v[i][0], v[i][0], v[i][1] * v[i][1]) + __builtin_elementwise_fma(v[i][2], v[i][2], v[i][3] * v[i][3]);
        a2[i] = sw32_add(s.x + s.y, sq.x + sq.y);
      }
      float sm, sq;
      sw32_split(row_sum(sw16_add(a2[0], a2[1])), sm, sq);
      const float mean = sm * (1.f / 512.f);
      const float var = fmaxf(sq * (1.f / 512.f) - mean * mean, 0.f);
      const float r1 = __builtin_amdgcn_rsqf(var + 1e-5f);
      const float sc = r1 * __builtin_amdgcn_rsqf(var * r1 * r1 + 1e-5f), of = -mean * sc;
      const unsigned SG = 18 * ROW, X_CS = 2 * STG;
#pragma unroll
      for (int i = 0; i < 2; ++i) {
        const float s1 = uf(__builtin_amdgcn_readlane(fu(sc), 16 * i)), o1 = uf(__builtin_amdgcn_readlane(fu(of), 16 * i));
        const f32x2 s2 = {s1, s1}, o2 = {o1, o1};
        v4u hv;
#pragma unroll
        for (int e = 0; e < 4; ++e) hv[e] = pk2(__builtin_elementwise_fma(v[i][e], s2, o2));
        const int rho = 2 * w + i;
        lds_w128(SG + rho * 1024 + ((lane * 16) ^ ((rho & 3) << 6)), hv);
      }
      bar();
      if (w < 4) {
#pragma unroll
        for (int i = 0; i < 4; ++i) {
          const unsigned a = SG + (8 * hh + q) * 1024 + ((256 * w + 64 * i + 32 * blk + 8 * pp) ^ (q << 6));
          st_sc1(trfrag(a, a + 4096), rs, lane * 16 + (i & 1) * 1024, WS_H + ((2 * w + (i >> 1)) * 128 + jb) * 2048);
        }
      }
      if ((lane & 47) == 0) st_sc1(v4u{split2(-mean), (unsigned)tag, fu(r1), (unsigned)tag}, rs, blk * 16, WS_ST + (16 * jb + 2 * w) * 16);
      {
        const v4u cw[2] = {lds_r128(STG + w * 2048 + lane * 16), lds_r128(STG + w * 2048 + 1024 + lane * 16)};
        f32x2 a[4];
#pragma unroll
        for (int e = 0; e < 4; ++e) a[e] = f32x2{bflo(cw[0][e]), bfhi(cw[0][e])} + f32x2{bflo(cw[1][e]), bfhi(cw[1][e])};
        const float c0 = dpp_add<0x128>(sw16_add(sw32_add(a[0].x, a[2].x), sw32_add(a[1].x, a[3].x)));
        const float c1 = dpp_add<0x128>(sw16_add(sw32_add(a[0].y, a[2].y), sw32_add(a[1].y, a[3].y)));
        lds_w64(X_CS + w * 256 + (8 * (lane & 7) + 4 * hh + 2 * blk) * 4, v2u{fu(c0), fu(c1)});
      }
      if (w >= 4) { issue(0, 0); issue(0, 1); }
      publish(p.ws, WS_HF + jb * FS, tag, tid, w < 4);
      if (w == 0) {
        const unsigned a0 = X_CS + lane * 4;
        const float c = ((lds_r32(a0) + lds_r32(a0 + 256)) + (lds_r32(a0 + 512) + lds_r32(a0 + 768))) +
                        ((lds_r32(a0 + 1024) + lds_r32(a0 + 1280)) + (lds_r32(a0 + 1536) + lds_r32(a0 + 1792)));
        if (csum) __builtin_amdgcn_raw_buffer_store_b64(v2u{split2(c), (unsigned)tag}, rs, lane * 32, WS_CS + ((i16 >> 2) * 512 + 64 * (4 * gx + sx)) * 32 + (i16 & 3) * 8, AUX_SC1);
        wait_lgkm0();
      }
    }
    {
      const unsigned tb = (8 * hh + q) * 384 + 32 * blk + 8 * pp, sw = (q >> 1) << 6;
      unsigned ta[3][3];
#pragma unroll
      for (int fb = 0; fb < 3; ++fb) {
        ta[0][fb] = opaque(tb + ((192 * wl + 64 * fb) ^ sw)); ta[1][fb] = opaque(ta[0][fb] + STG); ta[2][fb] = opaque(ta[0][fb] + 2 * STG);
      }
      v4u wr[32][3], hr[32];
      auto rd = [&](int i, int fb) {
        const int s = i >> 3;
        const unsigned a = ta[s == 1 ? 2 : s == 2 ? 1 : 0][fb] + (i & 7) * 6144;
        wr[i][fb] = trfrag(a, a + 1536);
      };
      const unsigned hb = WS_H + ((2 * dT + (wh >> 1)) * 128 + 32 * sx) * 2048 + (wh & 1) * 1024, hv = lane * 16;
      const int nht = w < 4 ? 2 : 1, ht0 = w < 4 ? 2 * w : 4 + w;
      v2u bb[2][2];
#pragma unroll
      for (int e = 0; e < 2; ++e) {
        const char* bp = p.b1 + (f0 + 16 * (ht0 + (e < nht ? e : 0)) + 4 * hh) * 2;
        bb[e][0] = *(const v2u*)bp; bb[e][1] = *(const v2u*)(bp + 16);
      }
      issue(1, 0); issue(1, 1);
      if (w == 3) poll(p.ws, WS_HF + rep + (32 * sx + r) * FS, tag);
      PIN(bb[0][0]); PIN(bb[0][1]); PIN(bb[1][0]); PIN(bb[1][1]);
      wait_vm<0>(); bar();
      f32x16 acc[3] = {zero, zero, zero};
      sfor<0, 4>([&](auto I) { constexpr int i = decltype(I)::v; ald_sc1<(i & 1) * 2048>(hr[i], rs, hv, hb + (((i & 30) + 8 * rot) & 31) * 2048); });
      rd(0, 0); rd(0, 1); rd(0, 2); rd(1, 0); rd(1, 1); rd(1, 2);
      SB();
      sfor<0, 32>([&](auto I) {
        constexpr int i = decltype(I)::v;
        if constexpr (i == 8) { wait_vm<b_wait_g(2, 8)>(); bar(); }
        if constexpr (i == 16) { p3_lanes(); wait_vm<b_wait_g(10, 16)>(); bar(); }
        if constexpr (i == 24) { bar(); }
        use_vm<b_wait_h(i, i)>(hr[i]);
        SB();
        acc[0] = mfma(wr[i][0], hr[i], acc[0]);
        SB();
        if constexpr (b_g(i) == 3) issue(2 + (i >> 3), (i >> 1) & 1);
        if constexpr (b_g(i) == 1) p3_issue(s3, i - 24, w);
        if constexpr (i + 2 < 32) rd(i + 2, 0);
        SB();
        acc[1] = mfma(wr[i][1], hr[i], acc[1]);
        SB();
        if constexpr (b_h(i) >= 1) { constexpr int j = b_hfirst(i); ald_sc1<(j & 1) * 2048>(hr[j], rs, hv, hb + (((j & 30) + 8 * rot) & 31) * 2048); }
        if constexpr (i + 2 < 32) rd(i + 2, 1);
        SB();
        acc[2] = mfma(wr[i][2], hr[i], acc[2]);
        SB();
        if constexpr (b_h(i) == 2) { constexpr int j = b_hfirst(i) + 1; ald_sc1<(j & 1) * 2048>(hr[j], rs, hv, hb + (((j & 30) + 8 * rot) & 31) * 2048); }
        if constexpr (i + 2 < 32) rd(i + 2, 2);
        SB();
      });
      const unsigned pb = WS_PB + (dT * 8 + fT) * 196608;
      if (wh != sx) {
#pragma unroll
        for (int fb = 0; fb < 3; ++fb) {
          st_sc1(pack8<0>(acc[fb]), rs, lane * 16, pb + ((wh * 4 + sx) * 12 + 6 * wl + 2 * fb) * 1024);
          st_sc1(pack8<8>(acc[fb]), rs, lane * 16 + 1024, pb + ((wh * 4 + sx) * 12 + 6 * wl + 2 * fb) * 1024);
        }
      } else {
#pragma unroll
        for (int fb = 0; fb < 3; ++fb)
#pragma unroll
          for (int qd = 0; qd < 4; ++qd)
            lds_w128(STG + wl * 12288 + (fb * 4 + qd) * 1024 + lane * 16, f4(acc[fb][4 * qd], acc[fb][4 * qd + 1], acc[fb][4 * qd + 2], acc[fb][4 * qd + 3]));
      }
      publish(p.ws, WS_PF + ((dT * 8 + fT) * 4 + sx) * FS, tag, tid);
      f32x2 own[2][4];
#pragma unroll
      for (int e = 0; e < 2; ++e) {
        const unsigned a = STG + (ht0 + (e < nht ? e : 0)) * 2048 + lane * 16;
        const v4u lo = lds_r128(a), hi = lds_r128(a + 1024);
        own[e][0] = f32x2{uf(lo.x), uf(lo.y)} + f32x2{bflo(bb[e][0].x), bfhi(bb[e][0].x)}; own[e][1] = f32x2{uf(lo.z), uf(lo.w)} + f32x2{bflo(bb[e][0].y), bfhi(bb[e][0].y)};
        own[e][2] = f32x2{uf(hi.x), uf(hi.y)} + f32x2{bflo(bb[e][1].x), bfhi(bb[e][1].x)}; own[e][3] = f32x2{uf(hi.z), uf(hi.w)} + f32x2{bflo(bb[e][1].y), bfhi(bb[e][1].y)};
        PIN(own[e][0]); PIN(own[e][1]); PIN(own[e][2]); PIN(own[e][3]);
      }
#pragma unroll
      for (int i = 8; i < 14; ++i) { p3_issue(s3, i, w); rd_wo(i - 8, i - 7); }
      rd_wo(6, 8);
      if (w == 7) poll(p.ws, WS_PF + rep + ((dT * 8 + fT) * 4 + (lane & 3)) * FS, tag);
      bar();
      v4u pp4[2][3];
#pragma unroll
      for (int e = 0; e < 2; ++e)
        if (e < nht) {
#pragma unroll
          for (int kq = 0; kq < 3; ++kq) pp4[e][kq] = ld_sc1(rs, lane * 16, pb + ((sx * 4 + ((sx + 1 + kq) & 3)) * 12 + ht0 + e) * 1024);
        }
      if (w >= 4) {
#pragma unroll
        for (int i = 14; i < 28; ++i) p3_issue(s3, i, w);
      }
#pragma unroll
      for (int e = 0; e < 2; ++e) {
        if (e < nht) {
          const int ht = ht0 + e;
          v4u uo;
#pragma unroll
          for (int j = 0; j < 4; ++j) {
            const f32x2 t = ((f32x2{bflo(pp4[e][0][j]), bfhi(pp4[e][0][j])} + f32x2{bflo(pp4[e][1][j]), bfhi(pp4[e][1][j])}) +
                             f32x2{bflo(pp4[e][2][j]), bfhi(pp4[e][2][j])}) + own[e][j];
            uo[j] = pk(gelu(t.x), gelu(t.y));
          }
          if (fT < 7 || ht >= 10) st_sc1(uo, rs, lane * 16, WS_U + (((2 * dT + (sx >> 1)) * 86 + (f0 >> 4) + ht) * 2 + (sx & 1)) * 1024);
        }
      }
      publish(p.ws, WS_UF + ((4 * dT + sx) * 8 + fT) * FS, tag, tid);
    }
    if (w < 4) {
#pragma unroll
      for (int i = 14; i < 28; ++i) p3_issue(s3, i, w);
    }
    rd_w2(8, 14);
    wait_vm<13>();
    bar();
    rd_w2(14, 15);
    p3_issue(s3, 28, w); p3_issue(s3, 29, w);
  }

  {
    relane();
    const int ks0 = 22 * wh;
    const int srow = 64 * st64 + (tid >> 3);
    const v4u xres = *(const v4u*)(p.x + (size_t)srow * 1024 + dt3 * 128 + (tid & 7) * 16);
    const float b2v = bflo(*(const unsigned short*)(p.b2 + srow * 2));
    const unsigned t0[2] = {opaque(s3.b0 + tb3), opaque(s3.b0 + (tb3 ^ 64))};
    const unsigned t1[2] = {opaque(s3.b1 + tb3), opaque(s3.b1 + (tb3 ^ 64))};
    const unsigned t2[2] = {opaque(s3.b2 + tb3), opaque(s3.b2 + (tb3 ^ 64))};
    v4u ar[8][2], ur[22];
    auto rd = [&](int i, int c) {
      const int j = i < 20 ? i : i - 20;
      const unsigned a = j < 8 ? t0[c] + j * ROW : j < 15 ? t1[c] + (j - 8) * ROW : t2[c] + (j - 15) * ROW;
      wr3[i][c] = trfrag(a, a + 1024);
    };
    auto ldu = [&](int k) {
      const int kk = k < s3.kmax ? k : s3.kmax;
      ur[k] = ld_sc1(rs, lane * 16, WS_U + (dt3 * 86 + ks0 + kk) * 2048 + wl * 1024);
    };
    auto ldo = [&](int i) {
      const unsigned so = WS_O + (st64 * 32 + 8 * wh + i) * 2048;
      ar[i][0] = ld_sc1(rs, lane * 16, so); ar[i][1] = ld_sc1(rs, lane * 16 + 1024, so);
    };
    f32x16 acc[2] = {zero, zero};
    const unsigned fo = WS_OF + rep + ((st64 >> 1) * 8 + (lane & 7)) * FS, W_SEL = roleA ? 16 * ROW : 4 * ROW;
    if (w == 7) {
      gu64* fp = (gu64*)(p.ws + (hh ? fo : WS_UF + rep + (dt3 * 16 + (lane & 15)) * FS));
      u64 m = 0;
      __builtin_amdgcn_s_setprio(3);
#pragma unroll 1
      for (unsigned spins = 0; spins < SPIN_CAP; ++spins) {
        m = __ballot(__hip_atomic_load(fp, __ATOMIC_RELAXED, __HIP_MEMORY_SCOPE_AGENT) == tag);
        if ((unsigned)m == 0xffffffffu) break;
        __builtin_amdgcn_s_sleep(1);
      }
      __builtin_amdgcn_s_setprio(0);
      __builtin_amdgcn_fence(__ATOMIC_ACQUIRE, "wavefront");
      lds_w32(W_SEL, uf((unsigned)(m >> 32)));
    }
    wait_vm<0>();
    bar();
    const bool oready = rfl(fu(lds_r32(W_SEL))) == 0xffffffffu;
#pragma unroll
    for (int k = 0; k < 4; ++k) ldu(k);
    SB();
#pragma unroll
    for (int k = 0; k < 22; ++k) {
      const int i = k + 8;
      if (k == OGATE) {
        if (!oready) { if (w == 7) poll(p.ws, fo, tag); bar(); }
      }
      if (k < 20 || wh < 3) {
        acc[0] = mfma(ur[k], wr3[i][0], acc[0]);
        SB();
        if (i + 3 >= 15 && i + 3 < 30) { rd(i + 3, 0); rd(i + 3, 1); }
        if (4 + 2 * k < 22) ldu(4 + 2 * k);
        SB();
        acc[1] = mfma(ur[k], wr3[i][1], acc[1]);
        SB();
        if (5 + 2 * k < 22) ldu(5 + 2 * k);
        if (k >= OGATE && k < OGATE + 8) ldo(k - OGATE);
        SB();
      }
    }
#pragma unroll
    for (int i = 0; i < 8; ++i) {
      acc[0] = mfma(wof[i], ar[i][0], acc[0]);
      acc[1] = mfma(wof[i], ar[i][1], acc[1]);
      SB();
    }
    bar();
#pragma unroll
    for (int c = 0; c < 2; ++c)
#pragma unroll
      for (int qd = 0; qd < 4; ++qd)
        lds_w128(wh * 17408 + (32 * c + r) * 272 + (32 * wl + 8 * qd + 4 * hh) * 4, f4(acc[c][4 * qd], acc[c][4 * qd + 1], acc[c][4 * qd + 2], acc[c][4 * qd + 3]));
    bar();
    {
      float s[8];
#pragma unroll
      for (int e = 0; e < 4; ++e) { s[2 * e] = bflo(xres[e]) + b2v; s[2 * e + 1] = bfhi(xres[e]) + b2v; }
#pragma unroll
      for (int ww = 0; ww < 4; ++ww) {
        const unsigned a = ww * 17408 + (tid >> 3) * 272 + (tid & 7) * 32;
        const v4u lo = lds_r128(a), hi = lds_r128(a + 16);
#pragma unroll
        for (int j = 0; j < 4; ++j) { s[j] += uf(lo[j]); s[4 + j] += uf(hi[j]); }
      }
      st_sc1(v4u{pk(s[0], s[1]), pk(s[2], s[3]), pk(s[4], s[5]), pk(s[6], s[7])}, __builtin_amdgcn_make_buffer_rsrc(p.out, 0, 2048 * 1024, 0x00020000),
             (tid >> 3) * 1024 + (tid & 7) * 16, st64 * 65536 + dt3 * 128);
    }
    if (b == 0 && tid == 0) __hip_atomic_store((gu64*)p.ws, tag, __ATOMIC_RELAXED, __HIP_MEMORY_SCOPE_AGENT);
  }
}
}

extern "C" void kernel_launch(void* const* d_in, const int* in_sizes, int n_in, void* d_out, int out_size, void* d_ws,
                              size_t ws_size, hipStream_t stream) {
  hk::Params p;
  p.x = (const char*)d_in[0]; p.wqkv = (const char*)d_in[1]; p.wphi = (const char*)d_in[2]; p.wo = (const char*)d_in[3];
  p.w1 = (const char*)d_in[5]; p.b1 = (const char*)d_in[6]; p.w2 = (const char*)d_in[7]; p.b2 = (const char*)d_in[8];
  p.out = (char*)d_out; p.ws = (char*)d_ws;
  (void)hipFuncSetAttribute((const void*)hk::fused, hipFuncAttributeMaxDynamicSharedMemorySize, hk::LDS_BYTES);
  hk::fused<<<256, 512, hk::LDS_BYTES, stream>>>(p);
}
```
